```python
import torch

def _softmax(x, axis=-1):
    return torch.softmax(x, dim=axis)
def _gelu(x):
    return torch.nn.functional.gelu(x, approximate="tanh")
def _silu(x):
    return torch.nn.functional.silu(x)
def _sigmoid(x):
    return torch.sigmoid(x)
def _layernorm(x, axis=-1, eps=1e-5):
    m = x.mean(dim=axis, keepdim=True)
    v = x.var(dim=axis, keepdim=True, unbiased=False)
    return (x - m) / torch.sqrt(v + eps)
def _scatter_add(tgt, idx, src, axis=0):
    return tgt.clone().index_add_(axis, idx, src)
def _segment_sum(src, seg, n):
    out = torch.zeros((n,) + src.shape[1:], dtype=src.dtype)
    return out.index_add_(0, seg, src)
def _pad(x, pw):
    flat = tuple(v for pair in reversed(pw) for v in pair)
    return torch.nn.functional.pad(x, flat)

kernel_name = 'gated_mix__grouped_kv_mqa__ssm_conv_s4d__prompt'

def setup_inputs():

    _w = (lambda a: (lambda t: t.to(torch.bfloat16) if t.is_floating_point() else t)(torch.as_tensor(a)))
    def _A_setup():
        B, S, D, H = 1, 2048, 512, 32
        Dh = D // H
        return {
            "x": _w((lambda *s: torch.randn(*s, dtype=torch.bfloat16) * 0.05)(B, S, D)),
            "wq": _w((lambda *s: torch.randn(*s, dtype=torch.bfloat16) * 0.8)(D, H * Dh)),
            "wk": _w((lambda *s: torch.randn(*s, dtype=torch.bfloat16) * 0.8)(D, Dh)),
            "wv": _w((lambda *s: torch.randn(*s, dtype=torch.bfloat16) * 0.05)(D, Dh)),
            "wo": _w((lambda *s: torch.randn(*s, dtype=torch.bfloat16) * 0.05)(H * Dh, D)),
        }
    _A_in = _A_setup()

    def _B_setup():
        Bsz, L, D, N = 1, 2048, 512, 128
        return {
            "u": _w((lambda *s: torch.randn(*s, dtype=torch.bfloat16) * 0.05)(Bsz, L, D)),
            "log_A_real": _w((lambda *s: torch.randn(*s, dtype=torch.bfloat16) * 6.4)(D, N) * 0.5),
            "A_imag": _w((lambda *s: torch.randn(*s, dtype=torch.bfloat16) * 0.05)(D, N) * 3.14),
            "C_real": _w((lambda *s: torch.randn(*s, dtype=torch.bfloat16) * 0.05)(D, N) / N ** 0.5),
            "C_imag": _w((lambda *s: torch.randn(*s, dtype=torch.bfloat16) * 0.05)(D, N) / N ** 0.5),
            "log_dt": _w((lambda *s: torch.randn(*s, dtype=torch.bfloat16) * 0.05)(D) * 0.5 - 4.0),
            "D_skip": _w((lambda *s: torch.randn(*s, dtype=torch.bfloat16) * 0.05)(D)),
        }
    _B_in = _B_setup()

    return {
        "A_x": _A_in["x"],
        "A_wq": _A_in["wq"],
        "A_wk": _A_in["wk"],
        "A_wv": _A_in["wv"],
        "A_wo": _A_in["wo"],
        "B_u": _B_in["u"],
        "B_log_A_real": _B_in["log_A_real"],
        "B_A_imag": _B_in["A_imag"],
        "B_C_real": _B_in["C_real"],
        "B_C_imag": _B_in["C_imag"],
        "B_log_dt": _B_in["log_dt"],
        "B_D_skip": _B_in["D_skip"],
        "gate_w": _w((lambda *s: torch.randn(*s, dtype=torch.bfloat16) * 0.05)(512) * 16.0),
    }

def reference(A_x, A_wq, A_wk, A_wv, A_wo, B_u, B_log_A_real, B_A_imag, B_C_real, B_C_imag, B_log_dt, B_D_skip, gate_w):
    def _A_ref(x, wq, wk, wv, wo):
        B, S, D = x.shape
        H = 32
        Dh = D // H
        q = (lambda x, *d: x.permute(*d))((x @ wq).reshape(B, S, H, Dh), 0, 2, 1, 3)
        k = x @ wk
        v = x @ wv
        scores = torch.einsum("bhqd,bkd->bhqk", q, k) * (Dh ** -0.5)
        p = _softmax(scores, axis=-1)
        o = torch.einsum("bhqk,bkd->bhqd", p, v)
        return (lambda x, *d: x.permute(*d))(o, 0, 2, 1, 3).reshape(B, S, H * Dh) @ wo

    def _B_ref(u, log_A_real, A_imag, C_real, C_imag, log_dt, D_skip):
        L = u.shape[1]
        dt = torch.exp(log_dt)
        A = -torch.exp(log_A_real) + 1j * A_imag
        C = (C_real + 1j * C_imag) * dt[:, None]
        pos = (lambda x, dt: x.to(dt))(torch.arange(L), log_dt.dtype)
        P = torch.exp((A * dt[:, None])[:, :, None] * pos[None, None, :])
        K = 2.0 * torch.einsum("dn,dnl->dl", C, P).real
        L2 = 2 * L
        u_t = (lambda x, *d: x.permute(*d))(u, 0, 2, 1)
        Kf = (lambda x, *a, **k: torch.fft.rfft(x.float(), *a, **k))(K, L2)
        Uf = (lambda x, *a, **k: torch.fft.rfft(x.float(), *a, **k))(u_t, L2)
        y = (lambda x, *a, **k: torch.fft.irfft(x, *a, **k).to(torch.bfloat16))(Uf * Kf[None, :, :], L2)[:, :, :L]
        y = (lambda x, *d: x.permute(*d))(y, 0, 2, 1)
        return y + u * D_skip[None, None, :]

    x = A_x
    g = _sigmoid((x * gate_w).sum(-1, keepdims=True))
    a = _A_ref(x, A_wq, A_wk, A_wv, A_wo)
    b = _B_ref(x, B_log_A_real, B_A_imag, B_C_real, B_C_imag, B_log_dt, B_D_skip)
    return g * a + (1 - g) * b
```

```cpp
#include <hip/hip_runtime.h>
#include <hip/hip_bf16.h>
#include <stdint.h>

typedef __bf16 bf16x8 __attribute__((ext_vector_type(8)));
typedef __bf16 bf16x16 __attribute__((ext_vector_type(16)));
typedef _Float16 f16x8 __attribute__((ext_vector_type(8)));
typedef float f32x16 __attribute__((ext_vector_type(16)));
typedef float f32x8 __attribute__((ext_vector_type(8)));
typedef float f32x4 __attribute__((ext_vector_type(4)));
typedef float f32x2 __attribute__((ext_vector_type(2)));
typedef unsigned u32x4 __attribute__((ext_vector_type(4)));
typedef unsigned u32x2 __attribute__((ext_vector_type(2)));
typedef short s16x4 __attribute__((ext_vector_type(4)));
typedef unsigned short u16;
#define LAS __attribute__((address_space(3)))
#define GAS __attribute__((address_space(1)))
typedef LAS char* lptr;
#define DEV __device__ __forceinline__

constexpr int S = 2048, D = 512, NH = 32, NST = 128;
constexpr float QSCALE = 0.25f * 1.4426950408889634f;

constexpr size_t WS_Q = 0;
constexpr size_t WS_KV = WS_Q + 2u * 1024 * 1024;
constexpr int KV_VOFF = 65536, KV_BYTES = 65536 + 128 * 528;
constexpr size_t WS_O = WS_KV + 256 * 1024;
constexpr size_t WS_B = WS_O + 2u * 1024 * 1024;
constexpr size_t WS_G = WS_B + 2u * 1024 * 1024;

DEV unsigned cvtpk(float lo, float hi) { unsigned r; asm("v_cvt_pk_bf16_f32 %0, %1, %2" : "=v"(r) : "v"(lo), "v"(hi)); return r; }
DEV unsigned cvtpkh(float lo, float hi) { unsigned r; asm("v_cvt_pk_f16_f32 %0, %1, %2" : "=v"(r) : "v"(lo), "v"(hi)); return r; }
DEV float bf2f(u16 v) { return __uint_as_float((unsigned)v << 16); }
DEV float bflo(unsigned v) { return __uint_as_float(v << 16); }
DEV float bfhi(unsigned v) { return __uint_as_float(v & 0xffff0000u); }
DEV float rbf(float v) { return bflo(cvtpk(v, v)); }
DEV void glds16(const void* g, lptr l) { __builtin_amdgcn_global_load_lds((const GAS void*)g, (LAS void*)l, 16, 0, 0); }
DEV u32x4 lds128(lptr p) { return *(LAS u32x4*)p; }
DEV u32x2 lds64(lptr p) { return *(LAS u32x2*)p; }
DEV u32x2 ldstr(lptr p) { return __builtin_bit_cast(u32x2, __builtin_amdgcn_ds_read_tr16_b64_v4i16((LAS s16x4*)p)); }
DEV f32x16 mfma(u32x4 a, u32x4 b, f32x16 c) { return __builtin_amdgcn_mfma_f32_32x32x16_bf16(__builtin_bit_cast(bf16x8, a), __builtin_bit_cast(bf16x8, b), c, 0, 0, 0); }
DEV f32x16 mfmah(u32x4 a, u32x4 b, f32x16 c) { return __builtin_amdgcn_mfma_f32_32x32x16_f16(__builtin_bit_cast(f16x8, a), __builtin_bit_cast(f16x8, b), c, 0, 0, 0); }
DEV f32x16 smfmac(u32x4 a, f32x8 b, f32x16 c, int idx) { return __builtin_amdgcn_smfmac_f32_32x32x32_bf16(__builtin_bit_cast(bf16x8, a), __builtin_bit_cast(bf16x16, b), c, idx, 0, 0); }
DEV u32x4 cat(u32x2 a, u32x2 b) { return (u32x4){a.x, a.y, b.x, b.y}; }
DEV void wait_all_bar() { asm volatile("s_waitcnt vmcnt(0) lgkmcnt(0)" ::: "memory"); __syncthreads(); }

struct Params {
  const u16 *x, *wq, *wk, *wv, *wo, *lar, *aim, *cre, *cim, *ldt, *dsk, *gw;
  u16* out; char* ws;
};

DEV void blk_map(int b, int& tg, int& hg) { int xcd = b & 7, idx = b >> 3; tg = 4 * xcd + (idx >> 3); hg = idx & 7; }

DEV void stage_w(const u16* w, int col0, lptr dst, int wave, int lane) {
#pragma unroll
  for (int i = 0; i < 8; ++i) {
    int p = wave * 8 + i, row = p * 8 + (lane >> 3), c = (lane & 7) ^ (((row >> 1) & 1) << 2);
    glds16(w + row * D + col0 + c * 8, dst + p * 1024);
  }
}

constexpr int P_XS = 0, P_XROW = 1040, P_WS = 64 * P_XROW, P_WKV = P_WS + 65536, P_LDS = P_WKV + 16384;
__global__ void __launch_bounds__(512, 2) k_proj(Params p) {
  extern __shared__ __attribute__((aligned(16))) char smem[];
  lptr L = (lptr)smem;
  const int tid = threadIdx.x, lane = tid & 63, wave = __builtin_amdgcn_readfirstlane(tid >> 6);
  const int n = lane & 31, h = lane >> 5;
  int tg, hg; blk_map(blockIdx.x, tg, hg);
  const u16* xt = p.x + (size_t)tg * 64 * D;
#pragma unroll
  for (int i = 0; i < 8; ++i) { int r = wave * 8 + i; glds16(xt + r * D + lane * 8, L + P_XS + r * P_XROW); }
  if (hg < 4) {
    const u16* wkv = (hg >> 1) ? p.wv : p.wk;
#pragma unroll
    for (int i = 0; i < 2; ++i) { int pc = wave * 2 + i; glds16(wkv + pc * 512 + lane * 8, L + P_WKV + pc * 1024); }
  }
  stage_w(p.wq, hg * 64, L + P_WS, wave, lane);
  wait_all_bar();
  const int q4 = (lane & 15) >> 2, p4 = lane & 3, blk = (lane >> 4) & 1;
  {
    int tok = 8 * hg + wave;
    u32x4 xv = lds128(L + P_XS + tok * P_XROW + lane * 16);
    u32x4 gv = *(const u32x4*)(p.gw + lane * 8);
    float s = 0.f;
#pragma unroll
    for (int i = 0; i < 4; ++i) { s += bflo(xv[i]) * bflo(gv[i]); s += bfhi(xv[i]) * bfhi(gv[i]); }
#pragma unroll
    for (int o = 32; o; o >>= 1) s += __shfl_xor(s, o);
    if (lane == 0) ((float*)(p.ws + WS_G))[tg * 64 + tok] = 1.f / (1.f + __expf(-s));
  }
  f32x16 kv = {};
  const int kvt = hg & 1, isV = hg >> 1;
  if (hg < 4) {
    lptr wb = L + P_WKV + (8 * h + q4) * 32 + 8 * p4;
    lptr xb = L + P_XS + (32 * kvt + n) * P_XROW + h * 16;
#pragma unroll
    for (int i = 0; i < 4; ++i) {
      int ks = wave * 4 + i;
      u32x4 wf = cat(ldstr(wb + ks * 512), ldstr(wb + ks * 512 + 128));
      u32x4 xf = lds128(xb + ks * 32);
      kv = isV ? mfma(xf, wf, kv) : mfma(wf, xf, kv);
    }
  }
  const int ct = wave & 1, tt = (wave >> 1) & 1, kh = wave >> 2;
  f32x16 acc = {};
  {
    lptr wb = L + P_WS + (256 * kh + 8 * h + q4) * 128 + ((64 * ct + 32 * blk + 8 * p4) ^ ((q4 >> 1) << 6));
    lptr xb = L + P_XS + (32 * tt + n) * P_XROW + kh * 512 + h * 16;
#pragma unroll
    for (int ks = 0; ks < 16; ++ks) {
      u32x4 wf = cat(ldstr(wb + ks * 2048), ldstr(wb + ks * 2048 + 512));
      u32x4 xf = lds128(xb + ks * 32);
      acc = mfma(wf, xf, acc);
    }
  }
  __syncthreads();
  {
    f32x4 s0, s1;
#pragma unroll
    for (int i = 0; i < 4; ++i) { s0[i] = kh ? acc[i] : acc[8 + i]; s1[i] = kh ? acc[4 + i] : acc[12 + i]; }
    *(LAS f32x4*)(L + wave * 2048 + lane * 16) = s0;
    *(LAS f32x4*)(L + wave * 2048 + 1024 + lane * 16) = s1;
    if (hg < 4) {
      const bool up = isV && (n >= 16);
#pragma unroll
      for (int i = 0; i < 4; ++i) { s0[i] = up ? kv[8 + i] : kv[i]; s1[i] = up ? kv[12 + i] : kv[4 + i]; }
      *(LAS f32x4*)(L + 16384 + wave * 2048 + lane * 16) = s0;
      *(LAS f32x4*)(L + 16384 + wave * 2048 + 1024 + lane * 16) = s1;
    }
  }
  __syncthreads();
  {
    f32x4 r0 = *(LAS f32x4*)(L + (wave ^ 4) * 2048 + lane * 16), r1 = *(LAS f32x4*)(L + (wave ^ 4) * 2048 + 1024 + lane * 16);
    float v[8];
#pragma unroll
    for (int i = 0; i < 4; ++i) { v[i] = ((kh ? acc[8 + i] : acc[i]) + r0[i]) * QSCALE; v[4 + i] = ((kh ? acc[12 + i] : acc[4 + i]) + r1[i]) * QSCALE; }
    u32x4 qf = {cvtpk(v[0], v[1]), cvtpk(v[2], v[3]), cvtpk(v[4], v[5]), cvtpk(v[6], v[7])};
    int head = 4 * hg + 2 * ct + kh, ttile = 2 * tg + tt;
    *(u32x4*)(p.ws + WS_Q + ((size_t)ttile * 32 + head) * 1024 + lane * 16) = qf;
  }
  if (hg < 4 && wave == 0) {
    f32x4 a0 = {}, a1 = {};
#pragma unroll
    for (int w = 0; w < 8; ++w) { a0 += *(LAS f32x4*)(L + 16384 + w * 2048 + lane * 16); a1 += *(LAS f32x4*)(L + 16384 + w * 2048 + 1024 + lane * 16); }
    u32x4 f = {cvtpk(a0[0], a0[1]), cvtpk(a0[2], a0[3]), cvtpk(a1[0], a1[1]), cvtpk(a1[2], a1[3])};
    int kt = 2 * tg + kvt;
    char* kvb = p.ws + WS_KV;
    if (!isV) {
      int pn = (n & 19) | ((n & 4) << 1) | ((n & 8) >> 1);
      *(u32x4*)(kvb + kt * 1024 + h * 512 + pn * 16) = f;
    } else {
      *(u32x4*)(kvb + KV_VOFF + (2 * kt + (n >> 4)) * 528 + h * 256 + (n & 15) * 16) = f;
      if (lane < 2) *(u32x4*)(kvb + KV_VOFF + (2 * kt + lane) * 528 + 512) = (u32x4){0x3f803f80u, 0x3f803f80u, 0x3f803f80u, 0x3f803f80u};
    }
  }
}

constexpr int T_ROW = 528, T_CH = 59 * T_ROW;
constexpr int S_TAB = 0, S_KF = 2 * T_CH, KF_CH = 96 * 80;
constexpr int S_U4 = S_KF + 2 * KF_CH, U4_CH = 2080 * 8;
constexpr int S_U2 = S_U4 + 2 * U4_CH, S_LDS = S_U2 + (2048 + 8) * 4;
DEV f32x2 cmul(f32x2 a, f32x2 b) { return (f32x2){a.x * b.x - a.y * b.y, a.x * b.y + a.y * b.x}; }
DEV f32x2 zpow(float rho, float om, float pos) {
  float e = __builtin_amdgcn_exp2f(rho * pos), f = __builtin_amdgcn_fractf(om * pos);
  return (f32x2){e * __builtin_amdgcn_cosf(f), e * __builtin_amdgcn_sinf(f)};
}
__global__ void __launch_bounds__(512, 2) k_ssm(Params p) {
  extern __shared__ __attribute__((aligned(16))) char smem[];
  lptr L = (lptr)smem;
  const int tid = threadIdx.x, lane = tid & 63, wave = __builtin_amdgcn_readfirstlane(tid >> 6);
  const int n = lane & 31, h = lane >> 5;
  const int ch0 = blockIdx.x * 2;
  unsigned ug[4];
#pragma unroll
  for (int i = 0; i < 4; ++i) ug[i] = *(const unsigned*)(p.x + (size_t)(tid + 512 * i) * D + ch0);
  const int c = wave >> 2, ch = ch0 + c, mode = (wave & 1) * 64 + lane, isA = (wave >> 1) & 1;
  const float dt = rbf(expf(bf2f(p.ldt[ch])));
  float kscale;
  {
    const float a = rbf(expf(bf2f(p.lar[ch * NST + mode])));
    const float rho = -(a * dt) * 1.4426950408889634f, om = (bf2f(p.aim[ch * NST + mode]) * dt) * 0.15915494309189535f;
    float cm = fmaxf(fmaxf(fabsf(bf2f(p.cre[ch * NST + lane])), fabsf(bf2f(p.cre[ch * NST + 64 + lane]))),
                     fmaxf(fabsf(bf2f(p.cim[ch * NST + lane])), fabsf(bf2f(p.cim[ch * NST + 64 + lane]))));
#pragma unroll
    for (int o = 32; o; o >>= 1) cm = fmaxf(cm, __shfl_xor(cm, o));
    unsigned eb = (__float_as_uint(cm) >> 23) & 0xffu; eb = eb < 1u ? 1u : (eb > 253u ? 253u : eb);
    const float up = __uint_as_float((254u - eb) << 23), dn = __uint_as_float(eb << 23);
    kscale = 2.f * dt * dn;
    lptr tb = L + S_TAB + c * T_CH + mode * 4;
    if (!isA) {
      f32x2 w8 = zpow(rho, om, 8.f), b = {1.f, 0.f};
#pragma unroll
      for (int j = 0; j < 32; ++j) { *(LAS unsigned*)(tb + (27 + j) * T_ROW) = cvtpkh(b.x, b.y); b = cmul(b, w8); }
    } else {
      f32x2 cc = {bf2f(p.cre[ch * NST + mode]) * up, bf2f(p.cim[ch * NST + mode]) * up};
      f32x2 z1 = zpow(rho, om, 1.f), z256 = zpow(rho, om, 256.f);
      f32x2 z2 = cmul(z1, z1), z4 = cmul(z2, z2), z8 = cmul(z4, z4);
      int row = 0;
#define PUT(v) do { *(LAS unsigned*)(tb + row * T_ROW) = cvtpkh((v).x, -(v).y); ++row; } while (0)
      f32x2 t = cc;
#pragma unroll
      for (int i = 0; i < 8; ++i) { PUT(t); t = cmul(t, z1); }
      f32x2 base = cmul(cc, z256); t = base;
#pragma unroll
      for (int i = 0; i < 5; ++i) { PUT(t); t = cmul(t, z2); }
#pragma unroll
      for (int s = 0; s < 2; ++s) { base = cmul(base, z256); PUT(base); t = cmul(base, z4); PUT(t); t = cmul(base, z8); PUT(t); }
#pragma unroll
      for (int s = 0; s < 4; ++s) { base = cmul(base, z256); PUT(base); t = cmul(base, z8); PUT(t); }
#undef PUT
    }
  }
#pragma unroll
  for (int i = 0; i < 4; ++i) *(LAS unsigned*)(L + S_U2 + (tid + 512 * i) * 4) = ug[i];
  if (tid < 8) *(LAS unsigned*)(L + S_U2 + (2048 + tid) * 4) = 0;
  if (tid < 320) { int cc2 = tid / 160, o = tid % 160; *(LAS u32x4*)(L + S_KF + cc2 * KF_CH + o * 16) = (u32x4){0, 0, 0, 0}; }
  __syncthreads();
  {
    u32x4 d0 = lds128(L + S_U2 + tid * 16), d1 = lds128(L + S_U2 + tid * 16 + 16);
    unsigned d[8] = {d0[0], d0[1], d0[2], d0[3], d1[0], d1[1], d1[2], d1[3]};
#pragma unroll
    for (int cc2 = 0; cc2 < 2; ++cc2) {
      const unsigned sel = cc2 ? 0x07060302u : 0x05040100u;
      unsigned P[6];
#pragma unroll
      for (int i = 0; i < 6; ++i) P[i] = __builtin_amdgcn_perm(d[i + 1], d[i], sel);
      lptr dst = L + S_U4 + cc2 * U4_CH + (32 + 4 * tid) * 8;
      *(LAS u32x4*)dst = (u32x4){P[0], P[2], P[1], P[3]};
      *(LAS u32x4*)(dst + 16) = (u32x4){P[2], P[4], P[3], P[5]};
      if (tid < 32) {
        int pp = tid - 32; unsigned e[4];
#pragma unroll
        for (int i = 0; i < 4; ++i) { int q = pp + i; unsigned v = q >= 0 ? *(LAS unsigned*)(L + S_U2 + q * 4) : 0u; e[i] = cc2 ? (v >> 16) : (v & 0xffffu); }
        *(LAS u32x2*)(L + S_U4 + cc2 * U4_CH + tid * 8) = (u32x2){e[0] | (e[1] << 16), e[2] | (e[3] << 16)};
      }
    }
  }
  if ((wave & 2) == 0) {
    const int rt = wave & 1;
    const int seg = 4 * rt + (n >> 3), e = 7 - (n & 7);
    int arow;
    if (seg == 0) arow = e;
    else if (seg == 1) arow = 8 + ((0x43222100u >> (4 * e)) & 7);
    else if (seg < 4) arow = 13 + 3 * (seg - 2) + ((0x22111000u >> (4 * e)) & 7);
    else arow = 19 + 2 * (seg - 4) + (e > 4 ? 1 : 0);
    lptr ab = L + S_TAB + c * T_CH + arow * T_ROW + h * 16;
    lptr bb = L + S_TAB + c * T_CH + (27 + n) * T_ROW + h * 16;
    f32x16 k = {};
#pragma unroll
    for (int ks = 0; ks < 16; ++ks) k = mfmah(lds128(ab + ks * 32), lds128(bb + ks * 32), k);
#pragma unroll
    for (int q = 0; q < 4; ++q) {
      float v0 = k[4 * q] * kscale, v1 = k[4 * q + 1] * kscale, v2 = k[4 * q + 2] * kscale, v3 = k[4 * q + 3] * kscale;
      if (rt == 1 && h == 0 && (n & 1)) v3 = v2;
      int lag8 = 256 * (4 * rt + q) + 8 * n;
      *(LAS u32x2*)(L + S_KF + c * KF_CH + (32 + (lag8 >> 5)) * 80 + (lag8 & 31) * 2 + h * 8) = (u32x2){cvtpk(v0, v1), cvtpk(v2, v3)};
    }
  }
  __syncthreads();
  {
    const int dq = wave & 3;
    f32x16 y0 = {}, y1 = {};
    lptr ub = L + S_U4 + c * U4_CH + (32 + n - 8 * h - 7) * 8;
    lptr kb = L + S_KF + c * KF_CH + (32 + n) * 80 + h * 16;
#pragma unroll 2
    for (int i = 0; i < 8; ++i) {
      const int dl = 8 * dq + i;
#pragma unroll
      for (int s = 0; s < 2; ++s) {
        lptr u0 = ub + (32 * dl - 16 * s) * 8;
        u32x4 ta = cat(lds64(u0), lds64(u0 + 32));
        u32x4 tb2 = cat(lds64(u0 + 8192), lds64(u0 + 8192 + 32));
        u32x4 k0 = lds128(kb - dl * 80 + s * 32), k1 = lds128(kb + (32 - dl) * 80 + s * 32);
        y0 = mfma(ta, k0, y0);
        y1 = mfma(ta, k1, y1);
        y1 = mfma(tb2, k0, y1);
      }
    }
    lptr rb = L + S_TAB + c * 24576;
#pragma unroll
    for (int part = 0; part < 4; ++part) {
      if (part != dq) {
        int slot = part * 3 + ((dq - part - 1) & 3);
        f32x4 a, b2;
#pragma unroll
        for (int i = 0; i < 4; ++i) { a[i] = (part >> 1) ? y1[8 * (part & 1) + i] : y0[8 * (part & 1) + i]; b2[i] = (part >> 1) ? y1[8 * (part & 1) + 4 + i] : y0[8 * (part & 1) + 4 + i]; }
        *(LAS f32x4*)(rb + slot * 2048 + lane * 16) = a;
        *(LAS f32x4*)(rb + slot * 2048 + 1024 + lane * 16) = b2;
      }
    }
    __syncthreads();
    f32x4 a, b2;
#pragma unroll
    for (int i = 0; i < 4; ++i) {
      float lo0 = (dq & 1) ? y0[8 + i] : y0[i], lo1 = (dq & 1) ? y1[8 + i] : y1[i];
      float hi0 = (dq & 1) ? y0[12 + i] : y0[4 + i], hi1 = (dq & 1) ? y1[12 + i] : y1[4 + i];
      a[i] = (dq >> 1) ? lo1 : lo0; b2[i] = (dq >> 1) ? hi1 : hi0;
    }
#pragma unroll
    for (int j = 0; j < 3; ++j) { a += *(LAS f32x4*)(rb + (dq * 3 + j) * 2048 + lane * 16); b2 += *(LAS f32x4*)(rb + (dq * 3 + j) * 2048 + 1024 + lane * 16); }
    const int iblk = 32 * (dq >> 1) + n, t0 = 32 * iblk + 16 * (dq & 1) + 4 * h;
    const float dsk = bf2f(p.dsk[ch]);
    u32x2 ua = lds64(L + S_U4 + c * U4_CH + (32 + t0) * 8), ub2 = lds64(L + S_U4 + c * U4_CH + (32 + t0 + 8) * 8);
    u32x2 o0 = {cvtpk(a[0] + bflo(ua.x) * dsk, a[1] + bfhi(ua.x) * dsk), cvtpk(a[2] + bflo(ua.y) * dsk, a[3] + bfhi(ua.y) * dsk)};
    u32x2 o1 = {cvtpk(b2[0] + bflo(ub2.x) * dsk, b2[1] + bfhi(ub2.x) * dsk), cvtpk(b2[2] + bflo(ub2.y) * dsk, b2[3] + bfhi(ub2.y) * dsk)};
    char* dst = p.ws + WS_B + ((size_t)(t0 >> 6) * 512 + ch) * 128 + (t0 & 63) * 2;
    *(u32x2*)dst = o0; *(u32x2*)(dst + 16) = o1;
  }
}

DEV f32x8 lo8(f32x16 v) { return __builtin_shufflevector(v, v, 0, 1, 2, 3, 4, 5, 6, 7); }
DEV f32x8 hi8(f32x16 v) { return __builtin_shufflevector(v, v, 8, 9, 10, 11, 12, 13, 14, 15); }
DEV float tilemax(f32x16 s) {
  float m = fmaxf(s[0], s[1]);
#pragma unroll
  for (int i = 2; i < 16; i += 2) m = fmaxf(fmaxf(m, s[i]), s[i + 1]);
  return m;
}
DEV float xhalf_max(float m) { auto r = __builtin_amdgcn_permlane32_swap(__float_as_uint(m), __float_as_uint(m), false, false); return fmaxf(__uint_as_float(r[0]), __uint_as_float(r[1])); }
__global__ void __launch_bounds__(512, 2) k_attn(Params p) {
  extern __shared__ __attribute__((aligned(16))) char smem[];
  lptr L = (lptr)smem;
  const int tid = threadIdx.x, lane = tid & 63, wave = __builtin_amdgcn_readfirstlane(tid >> 6);
  const int n = lane & 31, h = lane >> 5;
  int tg, hg; blk_map(blockIdx.x, tg, hg);
  const int head = 4 * hg + (wave & 3), ttile = 2 * tg + (wave >> 2);
  const u32x4 qf = *(const u32x4*)(p.ws + WS_Q + ((size_t)ttile * 32 + head) * 1024 + lane * 16);
  for (int pc = wave; pc < 130; pc += 8) glds16(p.ws + WS_KV + pc * 1024 + lane * 16, L + pc * 1024);
  wait_all_bar();
  lptr kb = L + lane * 16;
  lptr vb = L + KV_VOFF + (n < 16 ? h * 256 + n * 16 : 512);
  const int idx = 0xDDDD;
  float m = xhalf_max(tilemax(mfma(lds128(kb), qf, (f32x16){})));
  f32x16 o;
  for (int pass = 0;; ++pass) {
    f32x16 negm;
#pragma unroll
    for (int i = 0; i < 16; ++i) negm[i] = -m;
    o = (f32x16){};
    f32x16 s = mfma(lds128(kb), qf, negm);
#pragma unroll 2
    for (int kt = 0; kt < 64; ++kt) {
      u32x4 v0 = lds128(vb + kt * 1056), v1 = lds128(vb + kt * 1056 + 528);
      f32x16 sn = mfma(lds128(kb + ((kt + 1) & 63) * 1024), qf, negm);
#pragma unroll
      for (int i = 0; i < 16; ++i) s[i] = __builtin_amdgcn_exp2f(s[i]);
      o = smfmac(v0, lo8(s), o, idx);
      o = smfmac(v1, hi8(s), o, idx);
      s = sn;
    }
    float chk = fabsf(o[0]);
#pragma unroll
    for (int i = 1; i < 8; ++i) chk = fmaxf(chk, fabsf(o[i]));
    const bool ok = (o[8] < 0x1p100f) && (chk < __builtin_inff()) && (o[0] == o[0]);
    if (pass == 1 || __all(ok)) break;
    float mt = -__builtin_inff();
    for (int kt = 0; kt < 64; ++kt) mt = fmaxf(mt, tilemax(mfma(lds128(kb + kt * 1024), qf, (f32x16){})));
    m = xhalf_max(mt);
  }
  const float rl = 1.f / o[8];
  u32x4 of = {cvtpk(o[0] * rl, o[1] * rl), cvtpk(o[2] * rl, o[3] * rl), cvtpk(o[4] * rl, o[5] * rl), cvtpk(o[6] * rl, o[7] * rl)};
  *(u32x4*)(p.ws + WS_O + ((size_t)ttile * 32 + head) * 1024 + lane * 16) = of;
}

constexpr int F_W = 0, F_B = 65536, F_X = F_B + 8192, F_LDS = F_X + 16384;
__global__ void __launch_bounds__(512, 2) k_final(Params p) {
  extern __shared__ __attribute__((aligned(16))) char smem[];
  lptr L = (lptr)smem;
  const int tid = threadIdx.x, lane = tid & 63, wave = __builtin_amdgcn_readfirstlane(tid >> 6);
  const int n = lane & 31, h = lane >> 5;
  int tg, cg; blk_map(blockIdx.x, tg, cg);
  const int ct = wave & 1, tt = (wave >> 1) & 1, kh = wave >> 2;
  stage_w(p.wo, cg * 64, L + F_W, wave, lane);
  glds16(p.ws + WS_B + ((size_t)tg * 512 + cg * 64) * 128 + wave * 1024 + lane * 16, L + F_B + wave * 1024);
  u32x4 of[16];
  const char* ob = p.ws + WS_O + ((size_t)(2 * tg + tt) * 32 + 16 * kh) * 1024 + lane * 16;
#pragma unroll
  for (int i = 0; i < 16; ++i) of[i] = *(const u32x4*)(ob + i * 1024);
  const float g = ((const float*)(p.ws + WS_G))[tg * 64 + 32 * tt + n];
  wait_all_bar();
  const int q4 = (lane & 15) >> 2, p4 = lane & 3, blk = (lane >> 4) & 1;
  f32x16 acc = {};
  {
    lptr wb = L + F_W + (256 * kh + 4 * h + q4) * 128 + ((64 * ct + 32 * blk + 8 * p4) ^ ((q4 >> 1) << 6));
#pragma unroll
    for (int ks = 0; ks < 16; ++ks) {
      u32x4 wf = cat(ldstr(wb + ks * 2048), ldstr(wb + ks * 2048 + 1024));
      acc = mfma(wf, of[ks], acc);
    }
  }
  f32x4 s0, s1;
#pragma unroll
  for (int i = 0; i < 4; ++i) { s0[i] = kh ? acc[i] : acc[8 + i]; s1[i] = kh ? acc[4 + i] : acc[12 + i]; }
  *(LAS f32x4*)(L + F_X + wave * 2048 + lane * 16) = s0;
  *(LAS f32x4*)(L + F_X + wave * 2048 + 1024 + lane * 16) = s1;
  lptr bb = L + F_B + (32 * ct + 16 * kh + 4 * h + q4) * 128 + (32 * tt + 16 * blk + 4 * p4) * 2;
  u32x2 b0 = ldstr(bb), b1 = ldstr(bb + 1024);
  __syncthreads();
  f32x4 r0 = *(LAS f32x4*)(L + F_X + (wave ^ 4) * 2048 + lane * 16), r1 = *(LAS f32x4*)(L + F_X + (wave ^ 4) * 2048 + 1024 + lane * 16);
  float v[8];
#pragma unroll
  for (int i = 0; i < 4; ++i) { v[i] = (kh ? acc[8 + i] : acc[i]) + r0[i]; v[4 + i] = (kh ? acc[12 + i] : acc[4 + i]) + r1[i]; }
  const float g1 = 1.f - g;
  float bv[8] = {bflo(b0.x), bfhi(b0.x), bflo(b0.y), bfhi(b0.y), bflo(b1.x), bfhi(b1.x), bflo(b1.y), bfhi(b1.y)};
#pragma unroll
  for (int i = 0; i < 8; ++i) v[i] = g * v[i] + g1 * bv[i];
  unsigned a0 = cvtpk(v[0], v[1]), a1 = cvtpk(v[2], v[3]), c0 = cvtpk(v[4], v[5]), c1 = cvtpk(v[6], v[7]);
  auto x0 = __builtin_amdgcn_permlane32_swap(a0, c0, false, false);
  auto x1 = __builtin_amdgcn_permlane32_swap(a1, c1, false, false);
  u32x4 res = {x0[0], x1[0], x0[1], x1[1]};
  *(u32x4*)(p.out + (size_t)(tg * 64 + 32 * tt + n) * D + cg * 64 + 32 * ct + 16 * kh + 8 * h) = res;
}

extern "C" void kernel_launch(void* const* d_in, const int* in_sizes, int n_in, void* d_out, int out_size, void* d_ws, size_t ws_size, hipStream_t stream) {
  Params p;
  p.x = (const u16*)d_in[0]; p.wq = (const u16*)d_in[1]; p.wk = (const u16*)d_in[2]; p.wv = (const u16*)d_in[3]; p.wo = (const u16*)d_in[4];
  p.lar = (const u16*)d_in[6]; p.aim = (const u16*)d_in[7]; p.cre = (const u16*)d_in[8]; p.cim = (const u16*)d_in[9];
  p.ldt = (const u16*)d_in[10]; p.dsk = (const u16*)d_in[11]; p.gw = (const u16*)d_in[12];
  p.out = (u16*)d_out; p.ws = (char*)d_ws;
  (void)hipFuncSetAttribute((const void*)k_proj, hipFuncAttributeMaxDynamicSharedMemorySize, P_LDS);
  (void)hipFuncSetAttribute((const void*)k_ssm, hipFuncAttributeMaxDynamicSharedMemorySize, S_LDS);
  (void)hipFuncSetAttribute((const void*)k_attn, hipFuncAttributeMaxDynamicSharedMemorySize, KV_BYTES);
  (void)hipFuncSetAttribute((const void*)k_final, hipFuncAttributeMaxDynamicSharedMemorySize, F_LDS);
  k_proj<<<256, 512, P_LDS, stream>>>(p);
  k_ssm<<<256, 512, S_LDS, stream>>>(p);
  k_attn<<<256, 512, KV_BYTES, stream>>>(p);
  k_final<<<256, 512, F_LDS, stream>>>(p);
}
```

```cpp
#include <hip/hip_runtime.h>
#include <hip/hip_bf16.h>
#include <stdint.h>

typedef __bf16 bf16x8 __attribute__((ext_vector_type(8)));
typedef __bf16 bf16x2 __attribute__((ext_vector_type(2)));
typedef _Float16 f16x2 __attribute__((ext_vector_type(2)));
typedef _Float16 f16x8 __attribute__((ext_vector_type(8)));
typedef float f32x16 __attribute__((ext_vector_type(16)));
typedef float f32x4 __attribute__((ext_vector_type(4)));
typedef float f32x2 __attribute__((ext_vector_type(2)));
typedef unsigned u32x4 __attribute__((ext_vector_type(4)));
typedef unsigned u32x2 __attribute__((ext_vector_type(2)));
typedef short s16x4 __attribute__((ext_vector_type(4)));
typedef unsigned short u16;
typedef unsigned long long u64;
#define LAS __attribute__((address_space(3)))
#define GAS __attribute__((address_space(1)))
typedef LAS char* lptr;
typedef GAS u64 gu64;
#define DEV __device__ __forceinline__
#define RLX_AGENT __ATOMIC_RELAXED, __HIP_MEMORY_SCOPE_AGENT
#define PIN(x) asm volatile("" : "+v"(x))

constexpr int D = 512, NST = 128, AUX_SC1 = 16;
constexpr float LOG2E = 1.4426950408889634f, QSCALE = 0.25f * LOG2E;

constexpr int WS_E = 0, WS_FB = 8192, WS_F2 = 12288;
constexpr int FS = 16, WS_FO = 512 << 10;
constexpr int NR1 = 16, WS_F1 = 256 << 10, F1_REP = 256 * FS;
constexpr int KV_TS = 2080;
constexpr int WS_G = 49152;
constexpr int WS_KV = 65536;
constexpr int WS_XT = 1 << 20;
constexpr int WS_O = 3 << 20;
constexpr int WS_B = 5 << 20;
constexpr int WS_BYTES = 7 << 20;
constexpr int XROW = 1040, L_X = 0, L_XCH = 133120;
constexpr int L_T = 64 * XROW, T_ROW = 528, T_CH = 59 * T_ROW;
constexpr int L_WKV = 133120;
constexpr int L_UP = 133120, UP_CH = 5120;
constexpr int L_KW = 147136, KW_CP = 4160, KW_CH = 8320;
constexpr int L_CNT = 163776, L_G = 132096, LDS_ALL = 163840;
constexpr int F_W = 0, F_B = L_UP, F_X = 65536;

DEV unsigned cvtpk(float lo, float hi) { return __builtin_bit_cast(unsigned, __builtin_convertvector((f32x2){lo, hi}, bf16x2)); }
DEV unsigned cvtpkh(f32x2 v) { return __builtin_bit_cast(unsigned, __builtin_convertvector(v, f16x2)); }
DEV lptr opq(lptr p) { unsigned a = (unsigned)(uintptr_t)p; asm volatile("" : "+v"(a)); return (lptr)(uintptr_t)a; }
#define SBAR() __builtin_amdgcn_sched_barrier(0)
DEV float bflo(unsigned v) { return __uint_as_float(v << 16); }
DEV float bfhi(unsigned v) { return __uint_as_float(v & 0xffff0000u); }
DEV float rbf(float v) { return bflo(cvtpk(v, v)); }
DEV float expc(float x) { float hi = x * LOG2E, lo = __builtin_fmaf(x, LOG2E, -hi) + x * 1.925963033500236e-8f; float e = __builtin_amdgcn_exp2f(hi); return __builtin_fmaf(e, lo * 0.6931471805599453f, e); }
DEV u32x4 lds128(lptr p) { return *(LAS u32x4*)p; }
DEV u32x2 lds64(lptr p) { return *(LAS u32x2*)p; }
DEV u32x2 ldstr(lptr p) { return __builtin_bit_cast(u32x2, __builtin_amdgcn_ds_read_tr16_b64_v4i16((LAS s16x4*)p)); }
DEV f32x16 mfma(u32x4 a, u32x4 b, f32x16 c) { return __builtin_amdgcn_mfma_f32_32x32x16_bf16(__builtin_bit_cast(bf16x8, a), __builtin_bit_cast(bf16x8, b), c, 0, 0, 0); }
typedef __bf16 bf16x16 __attribute__((ext_vector_type(16)));
typedef float f32x8 __attribute__((ext_vector_type(8)));
DEV f32x16 smfmac(u32x4 a, f32x8 b, f32x16 c) { return __builtin_amdgcn_smfmac_f32_32x32x32_bf16(__builtin_bit_cast(bf16x8, a), __builtin_bit_cast(bf16x16, b), c, 0xDDDD, 0, 0); }
DEV f32x4 mfma16(u32x4 a, u32x4 b, f32x4 c) { return __builtin_amdgcn_mfma_f32_16x16x32_bf16(__builtin_bit_cast(bf16x8, a), __builtin_bit_cast(bf16x8, b), c, 0, 0, 0); }
DEV f32x16 mfmah(u32x4 a, u32x4 b, f32x16 c) { return __builtin_amdgcn_mfma_f32_32x32x16_f16(__builtin_bit_cast(f16x8, a), __builtin_bit_cast(f16x8, b), c, 0, 0, 0); }
DEV u32x4 cat(u32x2 a, u32x2 b) { return (u32x4){a.x, a.y, b.x, b.y}; }
DEV void bar() { asm volatile("s_waitcnt lgkmcnt(0)\n\ts_barrier" ::: "memory"); }
#define VMWAIT(n) asm volatile("s_waitcnt vmcnt(" #n ")" ::: "memory")
#define DMA4L(sbase, voff, m0v, SC) asm volatile("s_mov_b32 m0, %2\n\ts_nop 0\n\tglobal_load_lds_dwordx4 %0, %1" SC "\n\tglobal_load_lds_dwordx4 %0, %1 offset:1024" SC \
    "\n\tglobal_load_lds_dwordx4 %0, %1 offset:2048" SC "\n\tglobal_load_lds_dwordx4 %0, %1 offset:3072" SC :: "v"(voff), "s"(sbase), "s"(m0v) : "memory")
#define DMA2(sbase, voff, m0v, STEP, SC) asm volatile("s_mov_b32 m0, %2\n\ts_nop 0\n\tglobal_load_lds_dwordx4 %0, %1" SC "\n\t" \
    "s_add_u32 m0, m0, " #STEP "\n\ts_nop 0\n\tglobal_load_lds_dwordx4 %0, %1 offset:1024" SC :: "v"(voff), "s"(sbase), "s"(m0v) : "scc", "memory")
#define CNT_ADD(OFF, val) asm volatile("s_mov_b64 exec, 1\n\tds_add_u32 %0, %1 offset:" #OFF "\n\ts_mov_b64 exec, -1" :: "v"(cntb), "v"(val) : "memory")
#define CNT_ADD_RTN(old, OFF, val) do { unsigned r_; asm volatile("s_mov_b64 exec, 1\n\tds_add_rtn_u32 %0, %1, %2 offset:" #OFF "\n\ts_mov_b64 exec, -1\n\ts_waitcnt lgkmcnt(0)" \
    : "=&v"(r_) : "v"(cntb), "v"(val) : "memory"); old = __builtin_amdgcn_readfirstlane(r_); } while (0)
#define DMA3L(sbase, voff, m0v, SC) asm volatile("s_mov_b32 m0, %2\n\ts_nop 0\n\tglobal_load_lds_dwordx4 %0, %1" SC "\n\tglobal_load_lds_dwordx4 %0, %1 offset:1024" SC \
    "\n\tglobal_load_lds_dwordx4 %0, %1 offset:2048" SC :: "v"(voff), "s"(sbase), "s"(m0v) : "memory")
#define DMA1(sbase, voff, m0v) asm volatile("s_mov_b32 m0, %2\n\ts_nop 0\n\tglobal_load_lds_dwordx4 %0, %1 sc1" :: "v"(voff), "s"(sbase), "s"(m0v) : "memory")
DEV void lds_wait(lptr c, unsigned target) {
  while (__builtin_amdgcn_readfirstlane(__hip_atomic_load((LAS unsigned*)c, __ATOMIC_RELAXED, __HIP_MEMORY_SCOPE_WORKGROUP)) < target) __builtin_amdgcn_s_sleep(1);
  asm volatile("" ::: "memory");
}
DEV void lds_waitb(lptr c, int sh, unsigned target) {
  while (((__builtin_amdgcn_readfirstlane(__hip_atomic_load((LAS unsigned*)c, __ATOMIC_RELAXED, __HIP_MEMORY_SCOPE_WORKGROUP)) >> sh) & 255) < target) __builtin_amdgcn_s_sleep(1);
  asm volatile("" ::: "memory");
}
#define HSYNC(target) do { asm volatile("s_waitcnt lgkmcnt(0)" ::: "memory"); CNT_ADD(4, 1); lds_wait(L + L_CNT + 4, target); } while (0)
DEV unsigned gld32(const void* sbase, unsigned voff) { unsigned r; asm volatile("global_load_dword %0, %1, %2" : "=v"(r) : "v"(voff), "s"(sbase) : "memory"); return r; }
DEV u64 mix64(u64 z) { z += 0x9e3779b97f4a7c15ull; z = (z ^ (z >> 30)) * 0xbf58476d1ce4e5b9ull; z = (z ^ (z >> 27)) * 0x94d049bb133111ebull; return z ^ (z >> 31); }
DEV void wait_flag(__amdgpu_buffer_rsrc_t rs, unsigned off, u64 tag) {
#pragma unroll 1
  for (int spin = 0; spin < (1 << 16); ++spin) {
    if (__all(__builtin_bit_cast(u64, __builtin_amdgcn_raw_buffer_load_b64(rs, off, 0, AUX_SC1)) == tag)) break;
    __builtin_amdgcn_s_sleep(8);
    asm volatile("" ::: "memory");
  }
  asm volatile("" ::: "memory");
}
template <int NR> DEV void wait_rep(const char* f, int rep_bytes, u64 tag) {
  bool ok = false;
  unsigned slot = (unsigned)__builtin_amdgcn_s_memrealtime() >> 4;
#pragma unroll 1
  for (int spin = 0; spin < (1 << 16); ++spin) {
    ok |= __hip_atomic_load((gu64*)(f + (slot & (NR - 1)) * rep_bytes), RLX_AGENT) == tag;
    if (__all(ok)) break;
    unsigned now;
    while ((now = (unsigned)__builtin_amdgcn_s_memrealtime() >> 4) == slot) __builtin_amdgcn_s_sleep(1);
    slot = now;
  }
  asm volatile("" ::: "memory");
}
DEV f32x2 cmul(f32x2 a, f32x2 w) { f32x2 r = a.xx * w; return __builtin_elementwise_fma(a.yy, (f32x2){-w.y, w.x}, r); }
DEV f32x2 zpow(float rho, float om, float pos) {
  float e = __builtin_amdgcn_exp2f(rho * pos), f = __builtin_amdgcn_fractf(om * pos);
  return (f32x2){e * __builtin_amdgcn_cosf(f), e * __builtin_amdgcn_sinf(f)};
}
DEV float tilemax(f32x16 s) {
  float m = fmaxf(s[0], s[1]);
#pragma unroll
  for (int i = 2; i < 16; i += 2) m = fmaxf(fmaxf(m, s[i]), s[i + 1]);
  return m;
}
DEV float xhalf_max(float m) { auto r = __builtin_amdgcn_permlane32_swap(__float_as_uint(m), __float_as_uint(m), false, false); return fmaxf(__uint_as_float(r[0]), __uint_as_float(r[1])); }
template <int CTRL> DEV unsigned dppmax(unsigned v) { return max(v, (unsigned)__builtin_amdgcn_update_dpp(0, (int)v, CTRL, 0xf, 0xf, true)); }

struct Params {
  const u16 *x, *wq, *wk, *wv, *wo, *lar, *aim, *cre, *cim, *ldt, *dsk, *gw;
  u16* out; char* ws;
};

__global__ void __launch_bounds__(768, 3) k_fused(Params p) {
  unsigned junk;
  { u64 pc; asm volatile("s_getpc_b64 %0" : "=s"(pc)); junk = gld32((const void*)(pc & ~127ull), min((((blockIdx.x >> 3) & 7) * 18 + (threadIdx.x >> 5)) * 128u, 15872 - 128u)); }
  extern __shared__ __attribute__((aligned(16))) char smem[];
  lptr L = (lptr)smem;
  const unsigned L0 = (unsigned)(uintptr_t)L;
  const int tid = threadIdx.x, lane = tid & 63, wave = __builtin_amdgcn_readfirstlane(tid >> 6);
  const int n = lane & 31, h = lane >> 5, c16 = lane & 15, g4 = lane >> 4, q4 = c16 >> 2, p4 = lane & 3, blk = g4 & 1;
  const int bid = blockIdx.x, tg = 4 * (bid & 7) + (bid >> 6), hg = (bid >> 3) & 7;
  const int ch0 = 2 * bid;
  const __amdgpu_buffer_rsrc_t rs = __builtin_amdgcn_make_buffer_rsrc(p.ws, 0, WS_BYTES, 0x00020000);
  const int isV = hg & 1, sub = hg >> 1;
  const unsigned l16 = lane * 16, cntb = L0 + L_CNT;
  if (tid < 16) *(LAS unsigned*)(L + L_CNT + tid * 4) = 0;
  unsigned plar, paim, pcre, pcim; u32x4 xa0, xa1, xa2, xa3, ga0, ga1, ga2, ga3;
  asm volatile("s_cmp_lt_u32 %12, 8\n\ts_cbranch_scc1 1f\n\ts_setprio 3\n\ts_mov_b32 m0, %21\n\t"
               "global_load_dwordx4 %4, %19, %18\n\tglobal_load_dwordx4 %5, %19, %18 offset:64\n\tglobal_load_dwordx4 %6, %19, %18 offset:128\n\tglobal_load_dwordx4 %7, %19, %18 offset:192\n\t"
               "global_load_lds_dwordx4 %20, %22\n\tglobal_load_lds_dwordx4 %20, %22 offset:1024\n\t"
               "global_load_lds_dwordx4 %20, %22 offset:2048\n\tglobal_load_lds_dwordx4 %20, %22 offset:3072\n\t"
               "global_load_dword %0, %13, %14\n\tglobal_load_dword %1, %13, %15\n\tglobal_load_dword %2, %13, %16\n\tglobal_load_dword %3, %13, %17\n\t"
               "global_load_dwordx4 %8, %24, %23\n\tglobal_load_dwordx4 %9, %24, %23 offset:64\n\tglobal_load_dwordx4 %10, %24, %23 offset:128\n\tglobal_load_dwordx4 %11, %24, %23 offset:192\n1:"
               : "=&v"(plar), "=&v"(paim), "=&v"(pcre), "=&v"(pcim), "=&v"(xa0), "=&v"(xa1), "=&v"(xa2), "=&v"(xa3), "=&v"(ga0), "=&v"(ga1), "=&v"(ga2), "=&v"(ga3)
               : "s"(wave), "v"(ch0 * NST * 2 + (tid & 128) * 2 + lane * 4), "s"(p.lar), "s"(p.aim), "s"(p.cre), "s"(p.cim), "s"(p.x),
                 "v"(tg * 65536 + (sub * 16 + c16) * 1024 + (wave & 3) * 256 + g4 * 16), "v"((wave & 3) * 4096 + l16),
                 "s"(L0 + L_WKV + (wave & 3) * 4096), "s"(isV ? p.wv : p.wk), "s"(p.gw), "v"((wave & 3) * 256 + g4 * 16) : "scc", "memory");
  bar();
  if (wave >= 8)
    {
      unsigned sj;
      asm volatile("s_load_dword %0, %1, 0\n\ts_load_dword %0, %2, 0" : "=&s"(sj) : "s"(p.ldt + ch0), "s"(p.dsk + ch0) : "memory");
      lptr wb = opq(L + L_WKV + (wave - 8) * 4096 + (8 * g4 + q4) * 32 + 8 * p4);
      lptr xw = opq(L + L_X + (16 * sub + c16) * XROW + (wave - 8) * 256 + g4 * 16);
      asm volatile("s_waitcnt vmcnt(8)" : "+v"(xa0), "+v"(xa1), "+v"(xa2), "+v"(xa3) : "v"(junk) : "memory");
      u32x4 xa[4] = {xa0, xa1, xa2, xa3}, wa[4];
#pragma unroll
      for (int ks = 0; ks < 4; ++ks) wa[ks] = cat(ldstr(wb + ks * 1024), ldstr(wb + ks * 1024 + 128));
      f32x4 kv = {};
      if (isV) {
#pragma unroll
        for (int ks = 0; ks < 4; ++ks) kv = mfma16(xa[ks], wa[ks], kv);
      } else {
#pragma unroll
        for (int ks = 0; ks < 4; ++ks) kv = mfma16(wa[ks], xa[ks], kv);
      }
      *(LAS f32x4*)(L + L_WKV + (wave - 8) * 4096 + l16) = kv;
      CNT_ADD(12, 1);
#pragma unroll
      for (int ks = 0; ks < 4; ++ks) *(LAS u32x4*)(xw + ks * 64) = xa[ks];
      asm volatile("s_waitcnt vmcnt(0)" : "+v"(ga0), "+v"(ga1), "+v"(ga2), "+v"(ga3) :: "memory");
      const f32x4 gl = mfma16(ga3, xa[3], mfma16(ga2, xa[2], mfma16(ga1, xa[1], mfma16(ga0, xa[0], (f32x4){}))));
      *(LAS float*)(L + L_G + (wave - 8) * 256 + lane * 4) = gl[0];
      asm volatile("s_waitcnt lgkmcnt(0)" :: "s"(sj) : "memory");
    }
  int role = wave; asm volatile("" : "+s"(role));
  if (role >= 8) {
    const int hw = wave - 8, sc = hw >> 1;
    unsigned ldt2, dsk2;
    asm volatile("s_load_dword %0, %2, 0\n\ts_load_dword %1, %3, 0\n\ts_waitcnt lgkmcnt(0)" : "=&s"(ldt2), "=&s"(dsk2) : "s"(p.ldt + ch0), "s"(p.dsk + ch0) : "memory");
    const float dt = rbf(expc(sc ? bfhi(ldt2) : bflo(ldt2)));
    asm volatile("s_waitcnt vmcnt(0)" : "+v"(plar), "+v"(paim), "+v"(pcre), "+v"(pcim) :: "memory");
    SBAR();
    unsigned tr[59]; float kscale;
    {
      const int sh = (hw & 1) ? 0 : 16;
      const float a = rbf(expc(bfhi(plar << sh)));
      const float rho = -(a * dt) * LOG2E, om = (bfhi(paim << sh) * dt) * -0.15915494309189535f;
      unsigned cm = max(max(pcre & 0x7fff0000u, (pcre << 16) & 0x7fff0000u), max(pcim & 0x7fff0000u, (pcim << 16) & 0x7fff0000u));
      cm = dppmax<0x111>(cm); cm = dppmax<0x112>(cm); cm = dppmax<0x114>(cm); cm = dppmax<0x118>(cm); cm = dppmax<0x142>(cm); cm = dppmax<0x143>(cm);
      unsigned eb = (unsigned)__builtin_amdgcn_readlane((int)cm, 63) >> 23; eb = eb < 1u ? 1u : (eb > 253u ? 253u : eb);
      kscale = 2.f * dt * __uint_as_float(eb << 23);
      const float up = __uint_as_float((254u - eb) << 23);
      f32x2 cc = {bfhi(pcre << sh) * up, -bfhi(pcim << sh) * up};
      f32x2 z1 = zpow(rho, om, 1.f), z8 = zpow(rho, om, 8.f), z256 = zpow(rho, om, 256.f);
      f32x2 z2 = cmul(z1, z1), z4 = cmul(z2, z2);
      f32x2 w8 = {z8.x, -z8.y}, w16 = cmul(w8, w8), b0 = {1.f, 0.f}, b1 = w8;
      f32x2 t = cc, base = cmul(cc, z256), u = base;
#pragma unroll
      for (int j = 0; j < 16; ++j) {
        tr[27 + 2 * j] = cvtpkh(b0); tr[28 + 2 * j] = cvtpkh(b1); b0 = cmul(b0, w16); b1 = cmul(b1, w16);
        if (j < 8) { tr[j] = cvtpkh(t); t = cmul(t, z1); }
        if (j < 5) { tr[8 + j] = cvtpkh(u); u = cmul(u, z2); }
        if (j >= 8 && j < 10) { base = cmul(base, z256); tr[13 + 3 * (j - 8)] = cvtpkh(base); tr[14 + 3 * (j - 8)] = cvtpkh(cmul(base, z4)); tr[15 + 3 * (j - 8)] = cvtpkh(cmul(base, z8)); }
        if (j >= 10 && j < 14) { base = cmul(base, z256); tr[19 + 2 * (j - 10)] = cvtpkh(base); tr[20 + 2 * (j - 10)] = cvtpkh(cmul(base, z8)); }
      }
#pragma unroll
      for (int j = 0; j < 59; ++j) PIN(tr[j]);
    }
    const int rt = hw & 1, cc = hw & 1, rho = hw >> 1;
    unsigned a_ab, a_bb, a_kw, a_ka, a_ub, a_ue, a_xt, a_bd, a_f2, a_fb, a_up, a_wo, a_sel;
    {
      const int seg = 4 * rt + (n >> 3), e = n & 7, pe = 32 + 16 * rho + c16 - 8 * g4 - 7;
      const unsigned pat = seg == 0 ? 0x76543210u : seg == 1 ? 0x43222100u : seg < 4 ? 0x22111000u : 0x11100000u;
      const int arow = (seg == 0 ? 0 : seg == 1 ? 8 : seg < 4 ? 7 + 3 * seg : 11 + 2 * seg) + ((pat >> (4 * e)) & 7);
      a_ab = L0 + L_T + sc * T_CH + arow * T_ROW + h * 16; a_bb = L0 + L_T + sc * T_CH + (27 + n) * T_ROW + h * 16;
      a_kw = L0 + L_KW + sc * KW_CH + (32 + 1024 * rt + 8 * n + 4 * h) * 2;
      a_ka = L0 + L_KW + cc * KW_CH + (pe & 1) * KW_CP + (pe >> 1) * 4;
      a_ub = L0 + L_UP + cc * UP_CH + (g4 >> 1) * 2560 + (16 + c16 - 3) * 32 + (g4 & 1) * 16;
      a_ue = L0 + L_UP + cc * UP_CH + rho * 2560 + (16 + c16) * 32 + (g4 >> 1) * 16 + ((g4 & 1) ^ 1) * 8;
      a_xt = WS_XT + (64 * hg + 16 * hw + c16) * 4096 + tg * 128 + g4 * 16;
      a_bd = WS_B + ((c16 >> 1) * 512 + ch0 + cc) * 128 + ((c16 & 1) * 32 + 16 * rho + 4 * g4) * 2;
      a_f2 = WS_F2 + (((n & 3) << 6) | ((bid >> 5) << 3) | (n >> 2)) * 8;
      a_fb = WS_FB + (hg * 32 + n) * 8;
      a_up = (lane >> 1) * 64 + (lane & 1) * 16;
      a_wo = ((lane >> 3) * D + hg * 64 + ((lane & 7) ^ ((lane >> 2) & 4)) * 8) * 2;
      a_sel = (rt && h && (n & 1)) ? 0x03020302u : 0x03020100u;
      asm volatile("" : "+v"(a_ab), "+v"(a_bb), "+v"(a_kw), "+v"(a_ka), "+v"(a_ub), "+v"(a_ue), "+v"(a_xt), "+v"(a_bd), "+v"(a_f2), "+v"(a_fb), "+v"(a_up), "+v"(a_wo), "+v"(a_sel));
    }
    lptr xb = opq(L + L_X + (8 * g4 + 3 - q4) * XROW + (64 * hg + 16 * hw + 4 * p4) * 2), gq = opq(L + L_G + lane * 4);
    bar();
    const u64 tag = *(LAS u64*)(L + L_CNT + 24);
    const u32x4 v0 = cat(ldstr(xb + 4 * XROW), ldstr(xb)), v1 = cat(ldstr(xb + 36 * XROW), ldstr(xb + 32 * XROW));
    float g0 = (*(LAS float*)gq + *(LAS float*)(gq + 256)) + (*(LAS float*)(gq + 512) + *(LAS float*)(gq + 768));
    PIN(g0);
    __builtin_amdgcn_s_sleep(8);
    wait_rep<NR1>(p.ws + WS_F1 + (hw * 64 + lane) * FS, F1_REP, tag);
    HSYNC(12);
    const char* kvs = p.ws + WS_KV;
    DMA4L(kvs + hw * 4096, l16, L0 + hw * 4096, " sc1");
    DMA2(kvs + 16384 + hw * 2048, l16, L0 + 16384 + hw * 2048, 0, " sc1");
    unsigned junk2;
    asm volatile("global_load_dword %0, %1, %2 sc1" : "=v"(junk2) : "v"(min(24576 + (((bid >> 3) * 4 + hw) * 7 + min(lane, 6)) * 128, 132992)), "s"(kvs) : "memory");
    SBAR();
    lds_wait(L + L_CNT + 16, 8);
    __builtin_amdgcn_s_setprio(0);
    asm volatile("s_mov_b32 m0, %0\n\ts_nop 0" :: "s"(L0 + L_T + sc * T_CH + (hw & 1) * 256 - 33792) : "memory");
#pragma unroll
    for (int j = 0; j < 59; ++j) asm volatile("ds_write_addtid_b32 %0 offset:%1" :: "v"(tr[j]), "i"(33792 + j * T_ROW) : "memory");
    __builtin_amdgcn_s_setprio(3);
    SBAR();
    {
      lptr ab = (lptr)(uintptr_t)a_ab, bb = (lptr)(uintptr_t)a_bb;
      VMWAIT(1);
      bar();
      DMA4L(kvs + 24576 + hw * 10240, l16, L0 + 24576 + hw * 10240, " sc1");
      DMA3L(kvs + 28672 + hw * 10240, l16, L0 + 28672 + hw * 10240, " sc1");
      DMA3L(kvs + 31744 + hw * 10240, l16, L0 + 31744 + hw * 10240, " sc1");
      u32x4 fa[16], fb[16];
      SBAR(); __builtin_amdgcn_s_setprio(0); SBAR();
#pragma unroll
      for (int ks = 0; ks < 16; ++ks) { fa[ks] = lds128(ab + ks * 32); fb[ks] = lds128(bb + ks * 32); }
      SBAR(); __builtin_amdgcn_s_setprio(3); SBAR();
      asm volatile("s_waitcnt vmcnt(0)" :: "v"(junk2) : "memory");
      CNT_ADD(0, 1);
      if (hw == 0 && !isV && lane < 16) __builtin_amdgcn_raw_buffer_store_b32(__float_as_uint(g0), rs, lane * 4, WS_G + (tg * 64 + sub * 16) * 4, AUX_SC1);
      if (rt == 0 && lane < 8) *(LAS u32x4*)(L + L_KW + sc * KW_CH + (lane >> 2) * KW_CP + (lane & 3) * 16) = (u32x4){0, 0, 0, 0};
      if (lane < 32) *(LAS u32x4*)(L + L_UP + hw * 2560 + lane * 16) = (u32x4){0, 0, 0, 0};
      __builtin_amdgcn_raw_buffer_store_b128(v0, rs, a_xt, 0, AUX_SC1);
      __builtin_amdgcn_raw_buffer_store_b128(v1, rs, a_xt + 64, 0, AUX_SC1);
      HSYNC(16);
#pragma unroll 1
      for (int r = 4; r < 8; ++r) DMA4L(kvs + r * 16384 + hw * 4096, l16, L0 + r * 16384 + hw * 4096, " sc1");
      if (hw < 2) DMA1(kvs + 131072 + hw * 1024, l16, L0 + 131072 + hw * 1024);
      VMWAIT(0);
      unsigned prev;
      CNT_ADD_RTN(prev, 0, 256);
      if ((prev >> 8) == 3 && lane == 0) {
        __hip_atomic_store((gu64*)(p.ws + WS_F2) + bid, tag, RLX_AGENT);
        if (bid == 0) __hip_atomic_store((gu64*)(p.ws + WS_E), tag, RLX_AGENT);
      }
      f32x16 k = {};
#pragma unroll
      for (int ks = 0; ks < 16; ++ks) { k = mfmah(fa[ks], fb[ks], k); SBAR(); __builtin_amdgcn_s_sleep(1); SBAR(); }
      lptr kw = (lptr)(uintptr_t)a_kw;
#pragma unroll
      for (int q = 0; q < 4; ++q) {
        const f32x2 v01 = (f32x2){k[4 * q], k[4 * q + 1]} * kscale, v23 = (f32x2){k[4 * q + 2], k[4 * q + 3]} * kscale;
        const unsigned c01 = cvtpk(v01.x, v01.y), a01 = __builtin_amdgcn_perm(c01, c01, a_sel), a23 = cvtpk(v23.x, v23.y);
        *(LAS u32x2*)(kw + q * 512) = (u32x2){a01, a23};
        *(volatile LAS u16*)(kw + q * 512 + KW_CP - 2) = (u16)a01;
        *(volatile LAS unsigned*)(kw + q * 512 + KW_CP) = cvtpk(v01.y, v23.x);
        *(volatile LAS u16*)(kw + q * 512 + KW_CP + 4) = (u16)(a23 >> 16);
      }
    }
    wait_flag(rs, a_f2, tag);
    {
      const char* src = p.ws + WS_XT + (ch0 + (hw >> 1)) * 4096 + (hw & 1) * 32;
      asm volatile("s_mov_b32 m0, %2\n\ts_nop 0\n\tglobal_load_lds_dwordx4 %0, %1 sc1\n\ts_sub_u32 m0, m0, 1024\n\ts_nop 0\n\tglobal_load_lds_dwordx4 %0, %1 offset:2048 sc1"
                   :: "v"(a_up), "s"(src), "s"(L0 + L_UP + hw * 2560 + 512) : "scc", "memory");
    }
    VMWAIT(0);
    HSYNC(20);
    {
      f32x4 y[4] = {};
      int tmp;
      asm volatile(
    "s_waitcnt lgkmcnt(0)\n"
    "s_setprio 0\n"
    "ds_read_b128 v[64:67], %5 offset:1632\n"
    "ds_read_b128 v[68:71], %5 offset:1120\n"
    "ds_read_b128 v[72:75], %5 offset:608\n"
    "ds_read_b128 v[76:79], %5 offset:96\n"
    "ds_read2_b32 v[60:61], %4 offset0:0 offset1:1\n"
    "ds_read2_b32 v[62:63], %4 offset0:2 offset1:3\n"
    "ds_read_b128 v[84:87], %5 offset:1600\n"
    "ds_read_b128 v[88:91], %5 offset:1088\n"
    "ds_read_b128 v[92:95], %5 offset:576\n"
    "ds_read_b128 v[96:99], %5 offset:64\n"
    "ds_read2_b32 v[80:81], %4 offset0:16 offset1:17\n"
    "ds_read2_b32 v[82:83], %4 offset0:18 offset1:19\n"
    "s_mov_b32 %6, 4\n"
    "1:\n"
    "s_waitcnt lgkmcnt(6)\n"
    "s_setprio 3\n"
    "v_mfma_f32_16x16x32_bf16 %0, v[60:63], v[64:67], %0\n"
    "s_setprio 0\n"
    "ds_read_b128 v[64:67], %5 offset:1568\n"
    "s_setprio 3\n"
    "v_mfma_f32_16x16x32_bf16 %1, v[60:63], v[68:71], %1\n"
    "s_setprio 0\n"
    "ds_read_b128 v[68:71], %5 offset:1056\n"
    "s_setprio 3\n"
    "v_mfma_f32_16x16x32_bf16 %2, v[60:63], v[72:75], %2\n"
    "s_setprio 0\n"
    "ds_read_b128 v[72:75], %5 offset:544\n"
    "s_setprio 3\n"
    "v_mfma_f32_16x16x32_bf16 %3, v[60:63], v[76:79], %3\n"
    "s_setprio 0\n"
    "ds_read_b128 v[76:79], %5 offset:32\n"
    "ds_read2_b32 v[60:61], %4 offset0:32 offset1:33\n"
    "ds_read2_b32 v[62:63], %4 offset0:34 offset1:35\n"
    "s_waitcnt lgkmcnt(6)\n"
    "s_setprio 3\n"
    "v_mfma_f32_16x16x32_bf16 %0, v[80:83], v[84:87], %0\n"
    "s_setprio 0\n"
    "ds_read_b128 v[84:87], %5 offset:1536\n"
    "s_setprio 3\n"
    "v_mfma_f32_16x16x32_bf16 %1, v[80:83], v[88:91], %1\n"
    "s_setprio 0\n"
    "ds_read_b128 v[88:91], %5 offset:1024\n"
    "s_setprio 3\n"
    "v_mfma_f32_16x16x32_bf16 %2, v[80:83], v[92:95], %2\n"
    "s_setprio 0\n"
    "ds_read_b128 v[92:95], %5 offset:512\n"
    "s_setprio 3\n"
    "v_mfma_f32_16x16x32_bf16 %3, v[80:83], v[96:99], %3\n"
    "s_setprio 0\n"
    "ds_read_b128 v[96:99], %5 offset:0\n"
    "ds_read2_b32 v[80:81], %4 offset0:48 offset1:49\n"
    "ds_read2_b32 v[82:83], %4 offset0:50 offset1:51\n"
    "s_setprio 3\n"
    "v_add_u32 %4, 256, %4\n"
    "v_add_u32 %5, 0xffffff80, %5\n"
    "s_sub_u32 %6, %6, 1\n"
    "s_cmp_lg_u32 %6, 0\n"
    "s_waitcnt lgkmcnt(6)\n"
    "s_setprio 3\n"
    "v_mfma_f32_16x16x32_bf16 %0, v[60:63], v[64:67], %0\n"
    "s_setprio 0\n"
    "ds_read_b128 v[64:67], %5 offset:1632\n"
    "s_setprio 3\n"
    "v_mfma_f32_16x16x32_bf16 %1, v[60:63], v[68:71], %1\n"
    "s_setprio 0\n"
    "ds_read_b128 v[68:71], %5 offset:1120\n"
    "s_setprio 3\n"
    "v_mfma_f32_16x16x32_bf16 %2, v[60:63], v[72:75], %2\n"
    "s_setprio 0\n"
    "ds_read_b128 v[72:75], %5 offset:608\n"
    "s_setprio 3\n"
    "v_mfma_f32_16x16x32_bf16 %3, v[60:63], v[76:79], %3\n"
    "s_setprio 0\n"
    "ds_read_b128 v[76:79], %5 offset:96\n"
    "ds_read2_b32 v[60:61], %4 offset0:0 offset1:1\n"
    "ds_read2_b32 v[62:63], %4 offset0:2 offset1:3\n"
    "s_waitcnt lgkmcnt(6)\n"
    "s_setprio 3\n"
    "v_mfma_f32_16x16x32_bf16 %0, v[80:83], v[84:87], %0\n"
    "s_setprio 0\n"
    "ds_read_b128 v[84:87], %5 offset:1600\n"
    "s_setprio 3\n"
    "v_mfma_f32_16x16x32_bf16 %1, v[80:83], v[88:91], %1\n"
    "s_setprio 0\n"
    "ds_read_b128 v[88:91], %5 offset:1088\n"
    "s_setprio 3\n"
    "v_mfma_f32_16x16x32_bf16 %2, v[80:83], v[92:95], %2\n"
    "s_setprio 0\n"
    "ds_read_b128 v[92:95], %5 offset:576\n"
    "s_setprio 3\n"
    "v_mfma_f32_16x16x32_bf16 %3, v[80:83], v[96:99], %3\n"
    "s_setprio 0\n"
    "ds_read_b128 v[96:99], %5 offset:64\n"
    "ds_read2_b32 v[80:81], %4 offset0:16 offset1:17\n"
    "ds_read2_b32 v[82:83], %4 offset0:18 offset1:19\n"
    "s_cbranch_scc1 1b\n"
    "s_waitcnt lgkmcnt(0)\n"
    "s_setprio 0\n"
    "ds_read_b128 v[64:67], %5 offset:1632\n"
    "ds_read_b128 v[68:71], %5 offset:1120\n"
    "ds_read_b128 v[72:75], %5 offset:608\n"
    "ds_read2_b32 v[60:61], %4 offset0:0 offset1:1\n"
    "ds_read2_b32 v[62:63], %4 offset0:2 offset1:3\n"
    "ds_read_b128 v[84:87], %5 offset:1600\n"
    "ds_read_b128 v[88:91], %5 offset:1088\n"
    "ds_read_b128 v[92:95], %5 offset:576\n"
    "ds_read2_b32 v[80:81], %4 offset0:16 offset1:17\n"
    "ds_read2_b32 v[82:83], %4 offset0:18 offset1:19\n"
    "s_mov_b32 %6, 4\n"
    "2:\n"
    "s_waitcnt lgkmcnt(5)\n"
    "s_setprio 3\n"
    "v_mfma_f32_16x16x32_bf16 %0, v[60:63], v[64:67], %0\n"
    "s_setprio 0\n"
    "ds_read_b128 v[64:67], %5 offset:1568\n"
    "s_setprio 3\n"
    "v_mfma_f32_16x16x32_bf16 %1, v[60:63], v[68:71], %1\n"
    "s_setprio 0\n"
    "ds_read_b128 v[68:71], %5 offset:1056\n"
    "s_setprio 3\n"
    "v_mfma_f32_16x16x32_bf16 %2, v[60:63], v[72:75], %2\n"
    "s_setprio 0\n"
    "ds_read_b128 v[72:75], %5 offset:544\n"
    "ds_read2_b32 v[60:61], %4 offset0:32 offset1:33\n"
    "ds_read2_b32 v[62:63], %4 offset0:34 offset1:35\n"
    "s_waitcnt lgkmcnt(5)\n"
    "s_setprio 3\n"
    "v_mfma_f32_16x16x32_bf16 %0, v[80:83], v[84:87], %0\n"
    "s_setprio 0\n"
    "ds_read_b128 v[84:87], %5 offset:1536\n"
    "s_setprio 3\n"
    "v_mfma_f32_16x16x32_bf16 %1, v[80:83], v[88:91], %1\n"
    "s_setprio 0\n"
    "ds_read_b128 v[88:91], %5 offset:1024\n"
    "s_setprio 3\n"
    "v_mfma_f32_16x16x32_bf16 %2, v[80:83], v[92:95], %2\n"
    "s_setprio 0\n"
    "ds_read_b128 v[92:95], %5 offset:512\n"
    "ds_read2_b32 v[80:81], %4 offset0:48 offset1:49\n"
    "ds_read2_b32 v[82:83], %4 offset0:50 offset1:51\n"
    "s_setprio 3\n"
    "v_add_u32 %4, 256, %4\n"
    "v_add_u32 %5, 0xffffff80, %5\n"
    "s_sub_u32 %6, %6, 1\n"
    "s_cmp_lg_u32 %6, 0\n"
    "s_waitcnt lgkmcnt(5)\n"
    "s_setprio 3\n"
    "v_mfma_f32_16x16x32_bf16 %0, v[60:63], v[64:67], %0\n"
    "s_setprio 0\n"
    "ds_read_b128 v[64:67], %5 offset:1632\n"
    "s_setprio 3\n"
    "v_mfma_f32_16x16x32_bf16 %1, v[60:63], v[68:71], %1\n"
    "s_setprio 0\n"
    "ds_read_b128 v[68:71], %5 offset:1120\n"
    "s_setprio 3\n"
    "v_mfma_f32_16x16x32_bf16 %2, v[60:63], v[72:75], %2\n"
    "s_setprio 0\n"
    "ds_read_b128 v[72:75], %5 offset:608\n"
    "ds_read2_b32 v[60:61], %4 offset0:0 offset1:1\n"
    "ds_read2_b32 v[62:63], %4 offset0:2 offset1:3\n"
    "s_waitcnt lgkmcnt(5)\n"
    "s_setprio 3\n"
    "v_mfma_f32_16x16x32_bf16 %0, v[80:83], v[84:87], %0\n"
    "s_setprio 0\n"
    "ds_read_b128 v[84:87], %5 offset:1600\n"
    "s_setprio 3\n"
    "v_mfma_f32_16x16x32_bf16 %1, v[80:83], v[88:91], %1\n"
    "s_setprio 0\n"
    "ds_read_b128 v[88:91], %5 offset:1088\n"
    "s_setprio 3\n"
    "v_mfma_f32_16x16x32_bf16 %2, v[80:83], v[92:95], %2\n"
    "s_setprio 0\n"
    "ds_read_b128 v[92:95], %5 offset:576\n"
    "ds_read2_b32 v[80:81], %4 offset0:16 offset1:17\n"
    "ds_read2_b32 v[82:83], %4 offset0:18 offset1:19\n"
    "s_cbranch_scc1 2b\n"
    "s_waitcnt lgkmcnt(0)\n"
    "s_setprio 0\n"
    "ds_read_b128 v[64:67], %5 offset:1632\n"
    "ds_read_b128 v[68:71], %5 offset:1120\n"
    "ds_read2_b32 v[60:61], %4 offset0:0 offset1:1\n"
    "ds_read2_b32 v[62:63], %4 offset0:2 offset1:3\n"
    "ds_read_b128 v[84:87], %5 offset:1600\n"
    "ds_read_b128 v[88:91], %5 offset:1088\n"
    "ds_read2_b32 v[80:81], %4 offset0:16 offset1:17\n"
    "ds_read2_b32 v[82:83], %4 offset0:18 offset1:19\n"
    "s_mov_b32 %6, 4\n"
    "3:\n"
    "s_waitcnt lgkmcnt(4)\n"
    "s_setprio 3\n"
    "v_mfma_f32_16x16x32_bf16 %0, v[60:63], v[64:67], %0\n"
    "s_setprio 0\n"
    "ds_read_b128 v[64:67], %5 offset:1568\n"
    "s_setprio 3\n"
    "v_mfma_f32_16x16x32_bf16 %1, v[60:63], v[68:71], %1\n"
    "s_setprio 0\n"
    "ds_read_b128 v[68:71], %5 offset:1056\n"
    "ds_read2_b32 v[60:61], %4 offset0:32 offset1:33\n"
    "ds_read2_b32 v[62:63], %4 offset0:34 offset1:35\n"
    "s_waitcnt lgkmcnt(4)\n"
    "s_setprio 3\n"
    "v_mfma_f32_16x16x32_bf16 %0, v[80:83], v[84:87], %0\n"
    "s_setprio 0\n"
    "ds_read_b128 v[84:87], %5 offset:1536\n"
    "s_setprio 3\n"
    "v_mfma_f32_16x16x32_bf16 %1, v[80:83], v[88:91], %1\n"
    "s_setprio 0\n"
    "ds_read_b128 v[88:91], %5 offset:1024\n"
    "ds_read2_b32 v[80:81], %4 offset0:48 offset1:49\n"
    "ds_read2_b32 v[82:83], %4 offset0:50 offset1:51\n"
    "s_setprio 3\n"
    "v_add_u32 %4, 256, %4\n"
    "v_add_u32 %5, 0xffffff80, %5\n"
    "s_sub_u32 %6, %6, 1\n"
    "s_cmp_lg_u32 %6, 0\n"
    "s_waitcnt lgkmcnt(4)\n"
    "s_setprio 3\n"
    "v_mfma_f32_16x16x32_bf16 %0, v[60:63], v[64:67], %0\n"
    "s_setprio 0\n"
    "ds_read_b128 v[64:67], %5 offset:1632\n"
    "s_setprio 3\n"
    "v_mfma_f32_16x16x32_bf16 %1, v[60:63], v[68:71], %1\n"
    "s_setprio 0\n"
    "ds_read_b128 v[68:71], %5 offset:1120\n"
    "ds_read2_b32 v[60:61], %4 offset0:0 offset1:1\n"
    "ds_read2_b32 v[62:63], %4 offset0:2 offset1:3\n"
    "s_waitcnt lgkmcnt(4)\n"
    "s_setprio 3\n"
    "v_mfma_f32_16x16x32_bf16 %0, v[80:83], v[84:87], %0\n"
    "s_setprio 0\n"
    "ds_read_b128 v[84:87], %5 offset:1600\n"
    "s_setprio 3\n"
    "v_mfma_f32_16x16x32_bf16 %1, v[80:83], v[88:91], %1\n"
    "s_setprio 0\n"
    "ds_read_b128 v[88:91], %5 offset:1088\n"
    "ds_read2_b32 v[80:81], %4 offset0:16 offset1:17\n"
    "ds_read2_b32 v[82:83], %4 offset0:18 offset1:19\n"
    "s_cbranch_scc1 3b\n"
    "s_waitcnt lgkmcnt(0)\n"
    "s_setprio 0\n"
    "ds_read_b128 v[64:67], %5 offset:1632\n"
    "ds_read2_b32 v[60:61], %4 offset0:0 offset1:1\n"
    "ds_read2_b32 v[62:63], %4 offset0:2 offset1:3\n"
    "ds_read_b128 v[84:87], %5 offset:1600\n"
    "ds_read2_b32 v[80:81], %4 offset0:16 offset1:17\n"
    "ds_read2_b32 v[82:83], %4 offset0:18 offset1:19\n"
    "s_mov_b32 %6, 4\n"
    "4:\n"
    "s_waitcnt lgkmcnt(3)\n"
    "s_setprio 3\n"
    "v_mfma_f32_16x16x32_bf16 %0, v[60:63], v[64:67], %0\n"
    "s_setprio 0\n"
    "ds_read_b128 v[64:67], %5 offset:1568\n"
    "ds_read2_b32 v[60:61], %4 offset0:32 offset1:33\n"
    "ds_read2_b32 v[62:63], %4 offset0:34 offset1:35\n"
    "s_waitcnt lgkmcnt(3)\n"
    "s_setprio 3\n"
    "v_mfma_f32_16x16x32_bf16 %0, v[80:83], v[84:87], %0\n"
    "s_setprio 0\n"
    "ds_read_b128 v[84:87], %5 offset:1536\n"
    "ds_read2_b32 v[80:81], %4 offset0:48 offset1:49\n"
    "ds_read2_b32 v[82:83], %4 offset0:50 offset1:51\n"
    "s_setprio 3\n"
    "v_add_u32 %4, 256, %4\n"
    "v_add_u32 %5, 0xffffff80, %5\n"
    "s_sub_u32 %6, %6, 1\n"
    "s_cmp_lg_u32 %6, 0\n"
    "s_waitcnt lgkmcnt(3)\n"
    "s_setprio 3\n"
    "v_mfma_f32_16x16x32_bf16 %0, v[60:63], v[64:67], %0\n"
    "s_setprio 0\n"
    "ds_read_b128 v[64:67], %5 offset:1632\n"
    "ds_read2_b32 v[60:61], %4 offset0:0 offset1:1\n"
    "ds_read2_b32 v[62:63], %4 offset0:2 offset1:3\n"
    "s_waitcnt lgkmcnt(3)\n"
    "s_setprio 3\n"
    "v_mfma_f32_16x16x32_bf16 %0, v[80:83], v[84:87], %0\n"
    "s_setprio 0\n"
    "ds_read_b128 v[84:87], %5 offset:1600\n"
    "ds_read2_b32 v[80:81], %4 offset0:16 offset1:17\n"
    "ds_read2_b32 v[82:83], %4 offset0:18 offset1:19\n"
    "s_cbranch_scc1 4b\n"
    "s_waitcnt lgkmcnt(0)\n"
    "s_nop 7\n"
          : "+v"(y[3]), "+v"(y[2]), "+v"(y[1]), "+v"(y[0]), "+v"(a_ka), "+v"(a_ub), "=&s"(tmp) :: "v60", "v61", "v62", "v63", "v64", "v65", "v66", "v67", "v68", "v69", "v70", "v71", "v72", "v73", "v74", "v75", "v76", "v77", "v78", "v79", "v80", "v81", "v82", "v83", "v84", "v85", "v86", "v87", "v88", "v89", "v90", "v91", "v92", "v93", "v94", "v95", "v96", "v97", "v98", "v99", "v100", "v101", "v102", "v103", "v104", "v105", "v106", "v107", "v108", "v109", "v110", "v111", "v112", "v113", "v114", "v115", "v116", "v117", "v118", "v119", "v120", "v121", "v122", "v123", "v124", "v125", "v126", "v127", "v128", "v129", "v130", "v131", "v132", "v133", "scc", "memory");
      __builtin_amdgcn_s_setprio(3);
      const float dsk = cc ? bfhi(dsk2) : bflo(dsk2);
      lptr ue = (lptr)(uintptr_t)a_ue;
#pragma unroll
      for (int g = 0; g < 4; ++g) {
        u32x2 e = lds64(ue + g * 512);
        __builtin_amdgcn_raw_buffer_store_b64((u32x2){cvtpk(y[g][0] + bfhi(e.y) * dsk, y[g][1] + bflo(e.y) * dsk), cvtpk(y[g][2] + bfhi(e.x) * dsk, y[g][3] + bflo(e.x) * dsk)}, rs, a_bd, g * 8 * 65536, AUX_SC1);
      }
      VMWAIT(0);
      unsigned prev;
      CNT_ADD_RTN(prev, 8, 1);
      if (prev == 3 && lane == 0) __hip_atomic_store((gu64*)(p.ws + WS_FB) + bid, tag, RLX_AGENT);
    }
    {
      lds_waitb(L + L_CNT + 20, 0, 8);
#pragma unroll 1
      for (int i = 0; i < 16; ++i)
        asm volatile("s_mov_b32 m0, %2\n\ts_nop 0\n\tglobal_load_lds_dwordx4 %0, %1" :: "v"(a_wo), "s"(p.wo + ((hw * 128 + i * 8 + hg * 64) & 511) * D), "s"(L0 + F_W + hw * 16384 + i * 1024) : "memory");
      wait_flag(rs, a_fb, tag);
      lds_wait(L + L_CNT + 8, 4);
      DMA2(p.ws + WS_B + (tg * 512 + hg * 64) * 128 + hw * 2048, l16, L0 + F_B + hw * 2048, 0, " sc1");
      VMWAIT(0);
      CNT_ADD(12, 256);
    }
    if (hw == 0 && (bid >> 3) < 2) { __builtin_amdgcn_fence(__ATOMIC_RELEASE, "agent"); VMWAIT(0); }
    __builtin_amdgcn_endpgm();
  }
  const int ct = wave & 1, tt = (wave >> 1) & 1, kh = wave >> 2;
  if (wave < 7) {
    const bool isx = wave < 3;
    const int r0 = (sub * 16 + 16 + wave * 16) & 63;
    const unsigned vo = isx ? tg * 65536 + r0 * 1024 + l16 : (((wave - 3) * 128 + (lane >> 3)) * D + hg * 64 + ((lane & 7) ^ ((lane >> 2) & 4)) * 8) * 2;
    const unsigned gs = isx ? 1024 : 8192, ls = isx ? XROW : 1024, lb = isx ? L0 + L_X + r0 * XROW : L0 + L_T + (wave - 3) * 16384;
#pragma unroll 1
    for (int i = 0; i < 4; ++i) {
      if (!isx && i == 1) lds_wait(L + L_CNT + 12, 5);
      const int q = ((i + tg) & 3) * 4;
      unsigned v = vo + q * gs;
      asm volatile("s_mov_b32 m0, %2\n\ts_nop 0\n\t"
                   "global_load_lds_dwordx4 %0, %1\n\ts_add_u32 m0, m0, %4\n\tv_add_u32 %0, %3, %0\n\tglobal_load_lds_dwordx4 %0, %1\n\ts_add_u32 m0, m0, %4\n\tv_add_u32 %0, %3, %0\n\t"
                   "global_load_lds_dwordx4 %0, %1\n\ts_add_u32 m0, m0, %4\n\tv_add_u32 %0, %3, %0\n\tglobal_load_lds_dwordx4 %0, %1"
                   : "+v"(v) : "s"(isx ? p.x : p.wq), "s"(lb + q * ls), "s"(gs), "s"(ls) : "scc", "memory");
    }
  } else {
    const u64 tg7 = mix64(__hip_atomic_load((gu64*)(p.ws + WS_E), RLX_AGENT));
    if (lane == 0) *(LAS u64*)(L + L_CNT + 24) = tg7;
    const int kt = 2 * tg + (sub >> 1);
    int off = WS_KV + kt * KV_TS + (isV ? 1024 + (sub & 1) * 528 + (g4 >> 1) * 256 + c16 * 16 + (g4 & 1) * 8
                                        : (g4 & 1) * 512 + ((sub & 1) * 16 + c16) * 16 + (g4 >> 1) * 8);
    if (isV && lane == 0) __builtin_amdgcn_raw_buffer_store_b128((u32x4){0x3f803f80u, 0x3f803f80u, 0x3f803f80u, 0x3f803f80u}, rs, off + 512, 0, AUX_SC1);
    lptr pp = opq(L + L_WKV + l16);
    PIN(off);
    __builtin_amdgcn_s_setprio(3);
    lds_wait(L + L_CNT + 12, 1);
    f32x4 p0, p1, p2, p3; unsigned fc;
    do {
      asm volatile("ds_read_b32 %0, %5 offset:12\n\tds_read_b128 %1, %6\n\tds_read_b128 %2, %6 offset:4096\n\tds_read_b128 %3, %6 offset:8192\n\tds_read_b128 %4, %6 offset:12288\n\ts_waitcnt lgkmcnt(0)"
                   : "=&v"(fc), "=&v"(p0), "=&v"(p1), "=&v"(p2), "=&v"(p3) : "v"(cntb), "v"((unsigned)(uintptr_t)pp) : "memory");
    } while (__builtin_amdgcn_readfirstlane(fc) < 4);
    const f32x4 kv = (p0 + p1) + (p2 + p3);
    __builtin_amdgcn_raw_buffer_store_b64((u32x2){cvtpk(kv[0], kv[1]), cvtpk(kv[2], kv[3])}, rs, off, 0, AUX_SC1);
    CNT_ADD(12, 1);
    asm volatile("s_waitcnt vmcnt(0)" :: "v"(junk) : "memory");
    if (lane < NR1) __hip_atomic_store((gu64*)(p.ws + WS_F1 + lane * F1_REP + bid * FS), tg7, RLX_AGENT);
  }
  __builtin_amdgcn_s_setprio(2);
  u32x4 qf; f32x16 o = {};
  unsigned kaddr = L0 + l16, vaddr = L0 + (n < 16 ? h * 256 + n * 16 : n == 16 ? 512 : 0x40000);
  int cnt = 0, tmp, idx = 0xDDDD; unsigned fl = L0 + L_CNT;
  PIN(o); PIN(kaddr); PIN(vaddr); PIN(idx); PIN(fl);
  {
    lptr xw = opq(L + L_XCH + wave * 2048 + l16); unsigned xr = L0 + L_XCH + (wave ^ 4) * 2048 + l16; PIN(xr);
    lptr fb = opq(L + L_X + (32 * tt + n) * XROW + kh * 512 + h * 16);
    lptr wb = opq(L + L_T + (256 * kh + 8 * h + q4) * 128 + ((64 * ct + 32 * (blk ^ kh) + 8 * p4) ^ ((q4 >> 1) << 6)));
    u32x4 xf[16], wf[16];
    if (wave < 7) asm volatile("s_waitcnt vmcnt(0)" :: "v"(junk) : "memory");
    bar();
#pragma unroll
    for (int ks = 0; ks < 16; ++ks) xf[ks] = lds128(fb + ks * 32);
    CNT_ADD(4, 1);
    __builtin_amdgcn_s_setprio(1);
    f32x16 acc = {};
#pragma unroll
    for (int ks = 0; ks < 16 + 5; ++ks) {
      if (ks < 16) wf[ks] = cat(ldstr(wb + ks * 2048), ldstr(wb + ks * 2048 + 512));
      if (ks == 15) CNT_ADD(16, 1);
      SBAR();
      if (ks >= 5) acc = mfma(wf[ks - 5], xf[ks - 5], acc);
      SBAR();
    }
    *(LAS f32x4*)xw = (f32x4){acc[8], acc[9], acc[10], acc[11]};
    *(LAS f32x4*)(xw + 1024) = (f32x4){acc[12], acc[13], acc[14], acc[15]};
    asm volatile("ds_write_b32 %0, %1" :: "v"(cntb + 32 + wave * 4), "v"(1) : "memory");
    f32x4 r0, r1; unsigned fx;
    do {
      asm volatile("ds_read_b32 %0, %3\n\tds_read_b128 %1, %4\n\tds_read_b128 %2, %4 offset:1024\n\ts_waitcnt lgkmcnt(0)"
                   : "=&v"(fx), "=&v"(r0), "=&v"(r1) : "v"(cntb + 32 + (wave ^ 4) * 4), "v"(xr) : "memory");
    } while (__builtin_amdgcn_readfirstlane(fx) == 0);
    qf = (u32x4){cvtpk((acc[0] + r0[0]) * QSCALE, (acc[1] + r0[1]) * QSCALE), cvtpk((acc[2] + r0[2]) * QSCALE, (acc[3] + r0[3]) * QSCALE),
                 cvtpk((acc[4] + r1[0]) * QSCALE, (acc[5] + r1[1]) * QSCALE), cvtpk((acc[6] + r1[2]) * QSCALE, (acc[7] + r1[3]) * QSCALE)};
    PIN(qf);
  }
  {
    bar();
    asm volatile(
    "ds_read_b128 v[60:63], %1\n"
    "ds_read_b128 v[106:109], %1 offset:2080\n"
    "ds_read_b128 v[64:67], %2 offset:1024\n"
    "v_mov_b32 v96, 0\n"
    "v_mov_b32 v97, 0\n"
    "v_mov_b32 v98, 0\n"
    "v_mov_b32 v99, 0\n"
    "v_mov_b32 v100, 0\n"
    "v_mov_b32 v101, 0\n"
    "v_mov_b32 v102, 0\n"
    "v_mov_b32 v103, 0\n"
    "v_mov_b32 v114, 0\n"
    "v_mov_b32 v115, 0\n"
    "v_mov_b32 v116, 0\n"
    "v_mov_b32 v117, 0\n"
    "s_waitcnt lgkmcnt(2)\n"
    "v_mfma_f32_32x32x16_bf16 v[72:87], v[60:63], %5, 0\n"
    "ds_read_b128 v[68:71], %2 offset:1552\n"
    "ds_read_b128 v[60:63], %1 offset:4160\n"
    "ds_read_b128 v[110:113], %2 offset:3104\n"
    "s_setprio 2\n"
    "s_nop 7\n"
    "v_max3_f32 v118, v72, v73, v74\n"
    "v_max3_f32 v119, v75, v76, v77\n"
    "v_max3_f32 v118, v118, v78, v79\n"
    "v_max3_f32 v119, v119, v80, v81\n"
    "v_max3_f32 v118, v118, v82, v83\n"
    "v_max3_f32 v119, v119, v84, v85\n"
    "v_max3_f32 v118, v118, v86, v87\n"
    "v_max_f32 v118, v118, v119\n"
    "v_mov_b32 v119, v118\n"
    "s_nop 1\n"
    "v_permlane32_swap_b32 v118, v119\n"
    "s_nop 1\n"
    "v_min_f32_e64 v118, -v118, -v119\n"
    "v_mov_b32 v119, v118\n"
    "v_pk_add_f32 v[72:73], v[72:73], v[118:119]\n"
    "v_pk_add_f32 v[74:75], v[74:75], v[118:119]\n"
    "v_pk_add_f32 v[76:77], v[76:77], v[118:119]\n"
    "v_pk_add_f32 v[78:79], v[78:79], v[118:119]\n"
    "v_pk_add_f32 v[80:81], v[80:81], v[118:119]\n"
    "v_pk_add_f32 v[82:83], v[82:83], v[118:119]\n"
    "v_pk_add_f32 v[84:85], v[84:85], v[118:119]\n"
    "v_pk_add_f32 v[86:87], v[86:87], v[118:119]\n"
    "v_pk_mov_b32 v[120:121], v[118:119], v[118:119] op_sel:[0,1]\n"
    "v_pk_mov_b32 v[122:123], v[118:119], v[118:119] op_sel:[0,1]\n"
    "v_pk_mov_b32 v[124:125], v[118:119], v[118:119] op_sel:[0,1]\n"
    "v_pk_mov_b32 v[126:127], v[118:119], v[118:119] op_sel:[0,1]\n"
    "v_pk_mov_b32 v[128:129], v[118:119], v[118:119] op_sel:[0,1]\n"
    "v_pk_mov_b32 v[130:131], v[118:119], v[118:119] op_sel:[0,1]\n"
    "v_pk_mov_b32 v[132:133], v[118:119], v[118:119] op_sel:[0,1]\n"
    "1:\n"
    "v_exp_f32 v72, v72\n"
    "v_exp_f32 v73, v73\n"
    "s_waitcnt lgkmcnt(3)\n"
    "v_smfmac_f32_32x32x32_bf16 %0, v[114:117], v[96:103], %7\n"
    "ds_read_b128 v[114:117], %2 offset:3632\n"
    "v_exp_f32 v74, v74\n"
    "v_exp_f32 v75, v75\n"
    "v_exp_f32 v76, v76\n"
    "v_exp_f32 v77, v77\n"
    "v_mfma_f32_32x32x16_bf16 v[88:103], v[106:109], %5, v[118:133]\n"
    "ds_read_b128 v[106:109], %1 offset:6240\n"
    "v_exp_f32 v78, v78\n"
    "v_exp_f32 v79, v79\n"
    "v_exp_f32 v80, v80\n"
    "v_exp_f32 v81, v81\n"
    "v_exp_f32 v82, v82\n"
    "v_smfmac_f32_32x32x32_bf16 %0, v[64:67], v[72:79], %7\n"
    "ds_read_b128 v[64:67], %2 offset:5184\n"
    "v_exp_f32 v83, v83\n"
    "v_exp_f32 v84, v84\n"
    "v_exp_f32 v85, v85\n"
    "v_exp_f32 v86, v86\n"
    "v_exp_f32 v87, v87\n"
    "v_exp_f32 v88, v88\n"
    "v_exp_f32 v89, v89\n"
    "s_waitcnt lgkmcnt(3)\n"
    "v_smfmac_f32_32x32x32_bf16 %0, v[68:71], v[80:87], %7\n"
    "ds_read_b128 v[68:71], %2 offset:5712\n"
    "v_exp_f32 v90, v90\n"
    "v_exp_f32 v91, v91\n"
    "v_exp_f32 v92, v92\n"
    "v_exp_f32 v93, v93\n"
    "v_mfma_f32_32x32x16_bf16 v[72:87], v[60:63], %5, v[118:133]\n"
    "ds_read_b128 v[60:63], %1 offset:8320\n"
    "v_exp_f32 v94, v94\n"
    "v_exp_f32 v95, v95\n"
    "v_exp_f32 v96, v96\n"
    "v_exp_f32 v97, v97\n"
    "v_exp_f32 v98, v98\n"
    "v_smfmac_f32_32x32x32_bf16 %0, v[110:113], v[88:95], %7\n"
    "ds_read_b128 v[110:113], %2 offset:7264\n"
    "v_exp_f32 v99, v99\n"
    "v_exp_f32 v100, v100\n"
    "v_exp_f32 v101, v101\n"
    "v_exp_f32 v102, v102\n"
    "v_exp_f32 v103, v103\n"
    "v_exp_f32 v72, v72\n"
    "v_exp_f32 v73, v73\n"
    "s_waitcnt lgkmcnt(3)\n"
    "v_smfmac_f32_32x32x32_bf16 %0, v[114:117], v[96:103], %7\n"
    "ds_read_b128 v[114:117], %2 offset:7792\n"
    "v_exp_f32 v74, v74\n"
    "v_exp_f32 v75, v75\n"
    "v_exp_f32 v76, v76\n"
    "v_exp_f32 v77, v77\n"
    "v_mfma_f32_32x32x16_bf16 v[88:103], v[106:109], %5, v[118:133]\n"
    "ds_read_b128 v[106:109], %1 offset:10400\n"
    "v_exp_f32 v78, v78\n"
    "v_exp_f32 v79, v79\n"
    "v_exp_f32 v80, v80\n"
    "v_exp_f32 v81, v81\n"
    "v_exp_f32 v82, v82\n"
    "v_smfmac_f32_32x32x32_bf16 %0, v[64:67], v[72:79], %7\n"
    "ds_read_b128 v[64:67], %2 offset:9344\n"
    "v_exp_f32 v83, v83\n"
    "v_exp_f32 v84, v84\n"
    "v_exp_f32 v85, v85\n"
    "v_exp_f32 v86, v86\n"
    "v_exp_f32 v87, v87\n"
    "v_exp_f32 v88, v88\n"
    "v_exp_f32 v89, v89\n"
    "s_waitcnt lgkmcnt(3)\n"
    "v_smfmac_f32_32x32x32_bf16 %0, v[68:71], v[80:87], %7\n"
    "ds_read_b128 v[68:71], %2 offset:9872\n"
    "v_exp_f32 v90, v90\n"
    "v_exp_f32 v91, v91\n"
    "v_exp_f32 v92, v92\n"
    "v_exp_f32 v93, v93\n"
    "v_mfma_f32_32x32x16_bf16 v[72:87], v[60:63], %5, v[118:133]\n"
    "ds_read_b128 v[60:63], %1 offset:12480\n"
    "v_exp_f32 v94, v94\n"
    "v_exp_f32 v95, v95\n"
    "v_exp_f32 v96, v96\n"
    "v_exp_f32 v97, v97\n"
    "v_exp_f32 v98, v98\n"
    "v_smfmac_f32_32x32x32_bf16 %0, v[110:113], v[88:95], %7\n"
    "ds_read_b128 v[110:113], %2 offset:11424\n"
    "v_exp_f32 v99, v99\n"
    "v_exp_f32 v100, v100\n"
    "v_exp_f32 v101, v101\n"
    "v_exp_f32 v102, v102\n"
    "v_exp_f32 v103, v103\n"
    "v_exp_f32 v72, v72\n"
    "v_exp_f32 v73, v73\n"
    "s_waitcnt lgkmcnt(3)\n"
    "v_smfmac_f32_32x32x32_bf16 %0, v[114:117], v[96:103], %7\n"
    "ds_read_b128 v[114:117], %2 offset:11952\n"
    "v_exp_f32 v74, v74\n"
    "v_exp_f32 v75, v75\n"
    "v_exp_f32 v76, v76\n"
    "v_exp_f32 v77, v77\n"
    "v_mfma_f32_32x32x16_bf16 v[88:103], v[106:109], %5, v[118:133]\n"
    "ds_read_b128 v[106:109], %1 offset:14560\n"
    "v_exp_f32 v78, v78\n"
    "v_exp_f32 v79, v79\n"
    "v_exp_f32 v80, v80\n"
    "v_exp_f32 v81, v81\n"
    "v_exp_f32 v82, v82\n"
    "v_smfmac_f32_32x32x32_bf16 %0, v[64:67], v[72:79], %7\n"
    "ds_read_b128 v[64:67], %2 offset:13504\n"
    "v_exp_f32 v83, v83\n"
    "v_exp_f32 v84, v84\n"
    "v_exp_f32 v85, v85\n"
    "v_exp_f32 v86, v86\n"
    "v_exp_f32 v87, v87\n"
    "v_exp_f32 v88, v88\n"
    "v_exp_f32 v89, v89\n"
    "s_waitcnt lgkmcnt(3)\n"
    "v_smfmac_f32_32x32x32_bf16 %0, v[68:71], v[80:87], %7\n"
    "ds_read_b128 v[68:71], %2 offset:14032\n"
    "v_exp_f32 v90, v90\n"
    "v_exp_f32 v91, v91\n"
    "v_exp_f32 v92, v92\n"
    "v_exp_f32 v93, v93\n"
    "v_mfma_f32_32x32x16_bf16 v[72:87], v[60:63], %5, v[118:133]\n"
    "ds_read_b128 v[60:63], %1 offset:16640\n"
    "v_exp_f32 v94, v94\n"
    "v_exp_f32 v95, v95\n"
    "v_exp_f32 v96, v96\n"
    "v_exp_f32 v97, v97\n"
    "v_exp_f32 v98, v98\n"
    "v_smfmac_f32_32x32x32_bf16 %0, v[110:113], v[88:95], %7\n"
    "ds_read_b128 v[110:113], %2 offset:15584\n"
    "v_exp_f32 v99, v99\n"
    "v_exp_f32 v100, v100\n"
    "v_exp_f32 v101, v101\n"
    "v_exp_f32 v102, v102\n"
    "v_exp_f32 v103, v103\n"
    "v_exp_f32 v72, v72\n"
    "v_exp_f32 v73, v73\n"
    "s_waitcnt lgkmcnt(3)\n"
    "v_smfmac_f32_32x32x32_bf16 %0, v[114:117], v[96:103], %7\n"
    "ds_read_b128 v[114:117], %2 offset:16112\n"
    "v_exp_f32 v74, v74\n"
    "v_exp_f32 v75, v75\n"
    "v_exp_f32 v76, v76\n"
    "v_exp_f32 v77, v77\n"
    "v_mfma_f32_32x32x16_bf16 v[88:103], v[106:109], %5, v[118:133]\n"
    "ds_read_b128 v[106:109], %1 offset:18720\n"
    "v_exp_f32 v78, v78\n"
    "v_exp_f32 v79, v79\n"
    "v_exp_f32 v80, v80\n"
    "v_exp_f32 v81, v81\n"
    "v_exp_f32 v82, v82\n"
    "v_smfmac_f32_32x32x32_bf16 %0, v[64:67], v[72:79], %7\n"
    "ds_read_b128 v[64:67], %2 offset:17664\n"
    "v_exp_f32 v83, v83\n"
    "v_exp_f32 v84, v84\n"
    "v_exp_f32 v85, v85\n"
    "v_exp_f32 v86, v86\n"
    "v_exp_f32 v87, v87\n"
    "v_exp_f32 v88, v88\n"
    "v_exp_f32 v89, v89\n"
    "s_waitcnt lgkmcnt(3)\n"
    "v_smfmac_f32_32x32x32_bf16 %0, v[68:71], v[80:87], %7\n"
    "ds_read_b128 v[68:71], %2 offset:18192\n"
    "v_exp_f32 v90, v90\n"
    "v_exp_f32 v91, v91\n"
    "v_exp_f32 v92, v92\n"
    "v_exp_f32 v93, v93\n"
    "v_mfma_f32_32x32x16_bf16 v[72:87], v[60:63], %5, v[118:133]\n"
    "ds_read_b128 v[60:63], %1 offset:20800\n"
    "v_exp_f32 v94, v94\n"
    "v_exp_f32 v95, v95\n"
    "v_exp_f32 v96, v96\n"
    "v_exp_f32 v97, v97\n"
    "v_exp_f32 v98, v98\n"
    "v_smfmac_f32_32x32x32_bf16 %0, v[110:113], v[88:95], %7\n"
    "ds_read_b128 v[110:113], %2 offset:19744\n"
    "v_add_u32 %1, 16640, %1\n"
    "v_add_u32 %2, 16640, %2\n"
    "s_add_u32 %3, %3, 1\n"
    "s_cmp_lt_u32 %3, 4\n"
    "v_exp_f32 v99, v99\n"
    "v_exp_f32 v100, v100\n"
    "v_exp_f32 v101, v101\n"
    "v_exp_f32 v102, v102\n"
    "v_exp_f32 v103, v103\n"
    "s_cbranch_scc1 2f\n"
    "s_cmp_eq_u32 %3, %6\n"
    "s_cbranch_scc1 6f\n"
    "s_cmp_eq_u32 %3, 4\n"
    "s_cbranch_scc1 7f\n"
    "s_cmp_lg_u32 %3, 8\n"
    "s_cbranch_scc1 1b\n"
    "s_branch 5f\n"
    "6:\n"
    "s_setprio 0\n"
    "s_branch 1b\n"
    "7:\n"
    "s_setprio 1\n"
    "s_mov_b64 exec, 1\n"
    "v_mov_b32 v104, 1\n"
    "ds_add_u32 %8, v104 offset:20\n"
    "s_mov_b64 exec, -1\n"
    "s_waitcnt lgkmcnt(0)\n"
    "s_branch 1b\n"
    "2:\n"
    "ds_read_b32 v104, %8\n"
    "s_waitcnt lgkmcnt(0)\n"
    "v_readfirstlane_b32 %4, v104\n"
    "s_bitcmp1_b32 %4, 10\n"
    "s_cbranch_scc1 1b\n"
    "s_cmp_eq_u32 %3, 3\n"
    "s_cbranch_scc1 3f\n"
    "s_bitcmp1_b32 %4, 2\n"
    "s_cbranch_scc1 1b\n"
    "3:\n"
    "s_sleep 1\n"
    "s_branch 2b\n"
    "5:\n"
    "s_waitcnt lgkmcnt(0)\n"
    "v_smfmac_f32_32x32x32_bf16 %0, v[114:117], v[96:103], %7\n"
    "s_setprio 0\n"
    "s_nop 10\n"
        : "+v"(o), "+v"(kaddr), "+v"(vaddr), "+s"(cnt), "=&s"(tmp)
        : "v"(qf), "s"(kh ? 99 : 7), "v"(idx), "v"(fl)
        : "v60", "v61", "v62", "v63", "v64", "v65", "v66", "v67", "v68", "v69", "v70", "v71", "v72", "v73", "v74", "v75", "v76", "v77", "v78", "v79", "v80", "v81", "v82", "v83", "v84", "v85", "v86", "v87", "v88", "v89", "v90", "v91", "v92", "v93", "v94", "v95", "v96", "v97", "v98", "v99", "v100", "v101", "v102", "v103", "v104", "v105", "v106", "v107", "v108", "v109", "v110", "v111", "v112", "v113", "v114", "v115", "v116", "v117", "v118", "v119", "v120", "v121", "v122", "v123", "v124", "v125", "v126", "v127", "v128", "v129", "v130", "v131", "v132", "v133", "scc", "memory");
    auto lr = __builtin_amdgcn_permlane32_swap(__float_as_uint(o[8]), __float_as_uint(o[8]), false, false);
    o[8] = __uint_as_float(lr[0]);
    const float chk = ((fabsf(o[0]) + fabsf(o[1])) + (fabsf(o[2]) + fabsf(o[3]))) + ((fabsf(o[4]) + fabsf(o[5])) + (fabsf(o[6]) + fabsf(o[7])));
    const bool ok = (o[8] < 0x1p100f) && (chk < __builtin_inff());
    if (__builtin_expect(!__all(ok), 0)) {
      float mt = -__builtin_inff();
#pragma unroll 1
      for (int kt = 0; kt < 64; ++kt) mt = fmaxf(mt, tilemax(mfma(__builtin_amdgcn_raw_buffer_load_b128(rs, WS_KV + kt * KV_TS + l16, 0, AUX_SC1), qf, (f32x16){})));
      mt = xhalf_max(mt);
      const int vo = WS_KV + 1024 + (n < 16 ? h * 256 + n * 16 : 512);
      o = (f32x16){};
#pragma unroll 1
      for (int kt = 0; kt < 64; ++kt) {
        f32x16 s = mfma(__builtin_amdgcn_raw_buffer_load_b128(rs, WS_KV + kt * KV_TS + l16, 0, AUX_SC1), qf, (f32x16){});
#pragma unroll
        for (int i = 0; i < 16; ++i) s[i] = __builtin_amdgcn_exp2f(s[i] - mt);
        o = smfmac(__builtin_amdgcn_raw_buffer_load_b128(rs, vo + kt * KV_TS, 0, AUX_SC1), __builtin_shufflevector(s, s, 0, 1, 2, 3, 4, 5, 6, 7), o);
        o = smfmac(__builtin_amdgcn_raw_buffer_load_b128(rs, vo + kt * KV_TS + 528, 0, AUX_SC1), __builtin_shufflevector(s, s, 8, 9, 10, 11, 12, 13, 14, 15), o);
      }
    }
  }
  const u64 tag = *(LAS u64*)(L + L_CNT + 24);
  {
    const int kq = 2 * ct + kh, oidx = ((2 * tg + tt) * 4 + kq) * 8;
    const float rl = __builtin_amdgcn_rcpf(o[8]);
    u32x4 of[8];
    of[0] = (u32x4){cvtpk(o[0] * rl, o[1] * rl), cvtpk(o[2] * rl, o[3] * rl), cvtpk(o[4] * rl, o[5] * rl), cvtpk(o[6] * rl, o[7] * rl)};
    __builtin_amdgcn_raw_buffer_store_b128(of[0], rs, l16, WS_O + (oidx + hg) * 1024, AUX_SC1);
    const int wo0 = F_W + (16 * kq + 4 * h + q4) * 128 + ((64 * ct + 32 * (blk ^ kh) + 8 * p4) ^ ((q4 >> 1) << 6));
    lptr wb0 = opq(L + wo0), wb1 = opq(L + (wo0 ^ 64));
    lptr bb = opq(L + F_B + (16 * kq + 4 * h + q4) * 128 + (32 * tt + 16 * blk + 4 * p4) * 2);
    lds_wait(L + L_CNT + 12, 4 * 256);
    u32x4 wa[8], wb[8];
#pragma unroll
    for (int ks = 0; ks < 3; ++ks) { wa[ks] = cat(ldstr(wb0 + ks * 8192), ldstr(wb0 + ks * 8192 + 1024)); wb[ks] = cat(ldstr(wb1 + ks * 8192), ldstr(wb1 + ks * 8192 + 1024)); }
    SBAR();
    VMWAIT(0);
    if (lane == 0) __hip_atomic_store((gu64*)(p.ws + WS_FO + (oidx + hg) * FS), tag, RLX_AGENT);
    unsigned junk3;
    asm volatile("global_load_dword %0, %1, %2 sc1" : "=v"(junk3) : "v"(l16), "s"(p.ws + WS_O + (oidx + hg) * 1024) : "memory");
    CNT_ADD(20, 256);
#pragma unroll
    for (int ks = 3; ks < 8; ++ks) { wa[ks] = cat(ldstr(wb0 + ks * 8192), ldstr(wb0 + ks * 8192 + 1024)); wb[ks] = cat(ldstr(wb1 + ks * 8192), ldstr(wb1 + ks * 8192 + 1024)); }
    const u32x2 b0 = ldstr(bb), b1 = ldstr(bb + 1024);
    f32x16 a1 = mfma(wb[0], of[0], (f32x16){}), a0 = mfma(wa[0], of[0], (f32x16){});
    float bv[8] = {bflo(b0.x), bfhi(b0.x), bflo(b0.y), bfhi(b0.y), bflo(b1.x), bfhi(b1.x), bflo(b1.y), bfhi(b1.y)};
    unsigned oo = ((tg * 64 + 32 * tt + n) * D + hg * 64 + 16 * kq + 8 * h) * 2;
    asm volatile("" : "+v"(bv[0]), "+v"(bv[1]), "+v"(bv[2]), "+v"(bv[3]), "+v"(bv[4]), "+v"(bv[5]), "+v"(bv[6]), "+v"(bv[7]), "+v"(oo), "+v"(a0), "+v"(a1));
    lds_wait(L + L_CNT + 20, 8 * 256);
    __builtin_amdgcn_s_sleep(4);
    wait_rep<1>(p.ws + WS_FO + (oidx + (lane & 7)) * FS, 0, tag);
    asm volatile("" :: "v"(junk3));
#pragma unroll
    for (int i = 1; i < 8; ++i) of[i] = __builtin_amdgcn_raw_buffer_load_b128(rs, l16, WS_O + (oidx + ((hg + i) & 7)) * 1024, AUX_SC1);
    const float gl = __uint_as_float(__builtin_amdgcn_raw_buffer_load_b32(rs, n * 4, WS_G + (tg * 64 + 32 * tt) * 4, AUX_SC1));
    SBAR();
#pragma unroll
    for (int ks = 1; ks < 8; ++ks) { a1 = mfma(wb[ks], of[ks], a1); a0 = mfma(wa[ks], of[ks], a0); }
    SBAR();
    float g = __builtin_amdgcn_rcpf(1.f + __expf(-gl));
#pragma unroll
    for (int i = 0; i < 8; ++i) { bv[i] *= 1.f - g; PIN(bv[i]); }
    lptr x1 = opq(L + F_X + (tt * 4 + (kq ^ 1)) * 6144 + l16), x2 = opq(L + F_X + (tt * 4 + (kq ^ 2)) * 6144 + 2048 + l16), x3 = opq(L + F_X + (tt * 4 + (kq ^ 3)) * 6144 + 4096 + l16);
    *(LAS f32x4*)x2 = (f32x4){a1[0], a1[1], a1[2], a1[3]}; *(LAS f32x4*)(x2 + 1024) = (f32x4){a1[4], a1[5], a1[6], a1[7]};
    *(LAS f32x4*)x3 = (f32x4){a1[8], a1[9], a1[10], a1[11]}; *(LAS f32x4*)(x3 + 1024) = (f32x4){a1[12], a1[13], a1[14], a1[15]};
    *(LAS f32x4*)x1 = (f32x4){a0[8], a0[9], a0[10], a0[11]}; *(LAS f32x4*)(x1 + 1024) = (f32x4){a0[12], a0[13], a0[14], a0[15]};
    lptr xr = opq(L + F_X + (tt * 4 + kq) * 6144 + l16);
    bar();
    const f32x4 r0 = ((f32x4){a0[0], a0[1], a0[2], a0[3]} + *(LAS f32x4*)xr) + (*(LAS f32x4*)(xr + 2048) + *(LAS f32x4*)(xr + 4096));
    const f32x4 r1 = ((f32x4){a0[4], a0[5], a0[6], a0[7]} + *(LAS f32x4*)(xr + 1024)) + (*(LAS f32x4*)(xr + 3072) + *(LAS f32x4*)(xr + 5120));
    auto y0 = __builtin_amdgcn_permlane32_swap(cvtpk(g * r0[0] + bv[0], g * r0[1] + bv[1]), cvtpk(g * r1[0] + bv[4], g * r1[1] + bv[5]), false, false);
    auto y1 = __builtin_amdgcn_permlane32_swap(cvtpk(g * r0[2] + bv[2], g * r0[3] + bv[3]), cvtpk(g * r1[2] + bv[6], g * r1[3] + bv[7]), false, false);
    *(u32x4*)((char*)p.out + oo) = (u32x4){y0[0], y1[0], y0[1], y1[1]};
  }
  __builtin_amdgcn_endpgm();
}

extern "C" void kernel_launch(void* const* d_in, const int* in_sizes, int n_in, void* d_out, int out_size, void* d_ws, size_t ws_size, hipStream_t stream) {
  Params p;
  p.x = (const u16*)d_in[0]; p.wq = (const u16*)d_in[1]; p.wk = (const u16*)d_in[2]; p.wv = (const u16*)d_in[3]; p.wo = (const u16*)d_in[4];
  p.lar = (const u16*)d_in[6]; p.aim = (const u16*)d_in[7]; p.cre = (const u16*)d_in[8]; p.cim = (const u16*)d_in[9];
  p.ldt = (const u16*)d_in[10]; p.dsk = (const u16*)d_in[11]; p.gw = (const u16*)d_in[12];
  p.out = (u16*)d_out; p.ws = (char*)d_ws;
  (void)hipFuncSetAttribute((const void*)k_fused, hipFuncAttributeMaxDynamicSharedMemorySize, LDS_ALL);
  k_fused<<<256, 768, LDS_ALL, stream>>>(p);
}
```
